```python
import torch

def _softmax(x, axis=-1):
    return torch.softmax(x, dim=axis)
def _gelu(x):
    return torch.nn.functional.gelu(x, approximate="tanh")
def _silu(x):
    return torch.nn.functional.silu(x)
def _sigmoid(x):
    return torch.sigmoid(x)
def _layernorm(x, axis=-1, eps=1e-5):
    m = x.mean(dim=axis, keepdim=True)
    v = x.var(dim=axis, keepdim=True, unbiased=False)
    return (x - m) / torch.sqrt(v + eps)
def _scatter_add(tgt, idx, src, axis=0):
    return tgt.clone().index_add_(axis, idx, src)
def _segment_sum(src, seg, n):
    out = torch.zeros((n,) + src.shape[1:], dtype=src.dtype)
    return out.index_add_(0, seg, src)
def _pad(x, pw):
    flat = tuple(v for pair in reversed(pw) for v in pair)
    return torch.nn.functional.pad(x, flat)

kernel_name = 'standalone__window_attn_swin_v2__vision'

def setup_inputs():

    _w = (lambda a: (lambda t: t.to(torch.bfloat16) if t.is_floating_point() else t)(torch.as_tensor(a)))
    win = 7
    H = W = (384 // win) * win
    C = 96
    nh = 4
    shift = win // 2
    coords = torch.stack(torch.meshgrid(torch.arange(win), torch.arange(win), indexing="ij"), -1).reshape(-1, 2)
    rel = coords[:, None, :] - coords[None, :, :]
    rel_idx = ((rel[..., 0] + win - 1) * (2 * win - 1) + (rel[..., 1] + win - 1)).to(torch.int64)
    tab = torch.stack(torch.meshgrid(torch.arange(1 - win, win), torch.arange(1 - win, win), indexing="ij"), -1).reshape(-1, 2).to(float)
    tab = (lambda _x: torch.sign(torch.as_tensor(_x)))(tab) * (lambda _x: torch.log1p(torch.as_tensor(_x)))(torch.abs(tab))
    img = torch.zeros((H, W), dtype=torch.int64)
    cnt = 0
    for hs in (slice(0, H - win), slice(H - win, H - shift), slice(H - shift, H)):
        for ws in (slice(0, W - win), slice(W - win, W - shift), slice(W - shift, W)):
            img[hs, ws] = cnt; cnt += 1
    mw = img.reshape(H // win, win, W // win, win).permute(0, 2, 1, 3).reshape(-1, win * win)
    mask = (mw[:, :, None] != mw[:, None, :]).to(float) * (-100.0)
    return {
        "x": _w((lambda *s: torch.randn(*s, dtype=torch.bfloat16) * 0.05)(1, H, W, C)),
        "wqkv": _w((lambda *s: torch.randn(*s, dtype=torch.bfloat16) * 0.8)(C, 3 * C)),
        "wo": _w((lambda *s: torch.randn(*s, dtype=torch.bfloat16) * 0.05)(C, C)),
        "logit_scale": _w((lambda *s: torch.randn(*s, dtype=torch.bfloat16) * 0.8)(nh)),
        "cpb_w1": _w((lambda *s: torch.randn(*s, dtype=torch.bfloat16) * 0.8)(2, 4 * nh)),
        "cpb_w2": _w((lambda *s: torch.randn(*s, dtype=torch.bfloat16) * 0.8)(4 * nh, nh)),
        "rel_coords": _w(tab),
        "rel_idx": _w(rel_idx),
        "attn_mask": _w(mask),
    }

def reference(x, wqkv, wo, logit_scale, cpb_w1, cpb_w2, rel_coords, rel_idx, attn_mask):
    B, H, W, C = x.shape
    win = 7
    nh = 4
    hd = C // nh
    shift = win // 2
    ww = win * win
    nH, nW = H // win, W // win
    xs = torch.roll(x, (-shift, -shift), (1, 2))
    xw = (lambda x, *d: x.permute(*d))(xs.reshape(B, nH, win, nW, win, C), 0, 1, 3, 2, 4, 5).reshape(B * nH * nW, ww, C)
    qkv = (lambda x, *d: x.permute(*d))((xw @ wqkv).reshape(B * nH * nW, ww, 3, nh, hd), 2, 0, 3, 1, 4)
    q, k, v = qkv[0], qkv[1], qkv[2]
    qn = q * (torch.einsum("bhqd,bhqd->bhq", q, q)[..., None] + 1e-12) ** -0.5
    kn = k * (torch.einsum("bhkd,bhkd->bhk", k, k)[..., None] + 1e-12) ** -0.5
    logits = torch.einsum("bhqd,bhkd->bhqk", qn, kn)
    tau = torch.exp(torch.clip(logit_scale, -100.0, 4.6052))
    logits = logits * tau[None, :, None, None]
    hcpb = rel_coords @ cpb_w1
    hcpb = torch.where(hcpb > 0, hcpb, hcpb * 0)
    bias_tab = 16.0 * _sigmoid(hcpb @ cpb_w2)
    bias = (lambda x, *d: x.permute(*d))(bias_tab[rel_idx], 2, 0, 1)
    logits = logits.reshape(B, nH * nW, nh, ww, ww) + bias[None, None, :, :, :] + attn_mask[None, :, None, :, :]
    p = _softmax(logits.reshape(B * nH * nW, nh, ww, ww), axis=-1)
    o = (lambda x, *d: x.permute(*d))(torch.einsum("bhqk,bhkd->bhqd", p, v), 0, 2, 1, 3).reshape(B * nH * nW, ww, C) @ wo
    o = (lambda x, *d: x.permute(*d))(o.reshape(B, nH, nW, win, win, C), 0, 1, 3, 2, 4, 5).reshape(B, H, W, C)
    o = torch.roll(o, (shift, shift), (1, 2))
    return _layernorm(o, axis=-1)
```

```cpp
#include <hip/hip_runtime.h>
#include <hip/hip_bf16.h>
#include <stdint.h>

static __device__ __forceinline__ float bf2f(uint16_t v){ return __uint_as_float(((uint32_t)v)<<16); }
static __device__ __forceinline__ uint16_t f2bf(float f){ __hip_bfloat16 b = __float2bfloat16(f); return *(uint16_t*)&b; }

constexpr int HW=378, C=96, NH=4, HD=24, WW=49, NW=54;

__global__ __launch_bounds__(256) void naive_kernel(const uint16_t* x, const uint16_t* wqkv, const uint16_t* wo,
    const uint16_t* ls, const uint16_t* w1, const uint16_t* w2, const uint16_t* rc, const int* ridx,
    const uint16_t* mask, uint16_t* out) {
  __shared__ float xs[WW*C];
  __shared__ float qkv[WW*3*C];
  __shared__ float tab[169*4];
  __shared__ float S[NH*WW*WW];
  int w = blockIdx.x, wi = w / NW, wj = w % NW, t = threadIdx.x;
  for (int i = t; i < WW*C; i += 256) {
    int tok = i / C, c = i % C;
    int r = (wi*7 + tok/7 + 3) % HW, cc = (wj*7 + tok%7 + 3) % HW;
    xs[i] = bf2f(x[(r*HW+cc)*C + c]);
  }
  for (int i = t; i < 169*4; i += 256) {
    int p = i / 4, h = i % 4; float acc = 0;
    for (int j = 0; j < 16; ++j) {
      float hv = bf2f(rc[p*2])*bf2f(w1[j]) + bf2f(rc[p*2+1])*bf2f(w1[16+j]);
      hv = hv > 0 ? hv : 0;
      acc += hv * bf2f(w2[j*4+h]);
    }
    tab[i] = 16.f / (1.f + expf(-acc));
  }
  __syncthreads();
  for (int i = t; i < WW*3*C; i += 256) {
    int tok = i / (3*C), n = i % (3*C); float acc = 0;
    for (int k = 0; k < C; ++k) acc += xs[tok*C+k] * bf2f(wqkv[k*3*C+n]);
    qkv[i] = acc;
  }
  __syncthreads();
  for (int i = t; i < WW*2*NH; i += 256) {
    int tok = i / (2*NH), r = i % (2*NH);
    float* p = &qkv[tok*3*C + r*HD]; float s = 0;
    for (int d = 0; d < HD; ++d) s += p[d]*p[d];
    s = rsqrtf(s + 1e-12f);
    for (int d = 0; d < HD; ++d) p[d] *= s;
  }
  __syncthreads();
  for (int i = t; i < NH*WW*WW; i += 256) {
    int h = i / (WW*WW), q = (i / WW) % WW, k = i % WW; float acc = 0;
    for (int d = 0; d < HD; ++d) acc += qkv[q*3*C + h*HD + d] * qkv[k*3*C + C + h*HD + d];
    float tau = expf(fminf(fmaxf(bf2f(ls[h]), -100.f), 4.6052f));
    S[i] = acc*tau + tab[ridx[q*WW+k]*4+h] + bf2f(mask[(size_t)w*WW*WW + q*WW + k]);
  }
  __syncthreads();
  for (int i = t; i < NH*WW; i += 256) {
    float* p = &S[i*WW]; float m = -INFINITY;
    for (int k = 0; k < WW; ++k) m = fmaxf(m, p[k]);
    float s = 0; for (int k = 0; k < WW; ++k) { p[k] = expf(p[k]-m); s += p[k]; }
    s = 1.f/s; for (int k = 0; k < WW; ++k) p[k] *= s;
  }
  __syncthreads();
  for (int i = t; i < WW*C; i += 256) {
    int q = i / C, c = i % C, h = c / HD; float acc = 0;
    for (int k = 0; k < WW; ++k) acc += S[(h*WW+q)*WW+k] * qkv[k*3*C + 2*C + c];
    xs[i] = acc;
  }
  __syncthreads();
  for (int i = t; i < WW*C; i += 256) {
    int q = i / C, n = i % C; float acc = 0;
    for (int k = 0; k < C; ++k) acc += xs[q*C+k] * bf2f(wo[k*C+n]);
    qkv[i] = acc;
  }
  __syncthreads();
  if (t < WW) {
    float* p = &qkv[t*C]; float m = 0; for (int c = 0; c < C; ++c) m += p[c]; m /= C;
    float v = 0; for (int c = 0; c < C; ++c) v += (p[c]-m)*(p[c]-m); v /= C;
    float rs = rsqrtf(v + 1e-5f);
    int r = (wi*7 + t/7 + 3) % HW, cc = (wj*7 + t%7 + 3) % HW;
    for (int c = 0; c < C; ++c) out[(r*HW+cc)*C + c] = f2bf((p[c]-m)*rs);
  }
}

extern "C" void kernel_launch(void* const* d_in, const int* in_sizes, int n_in,
                              void* d_out, int out_size, void* d_ws, size_t ws_size,
                              hipStream_t stream) {
  naive_kernel<<<NW*NW, 256, 0, stream>>>((const uint16_t*)d_in[0], (const uint16_t*)d_in[1], (const uint16_t*)d_in[2],
    (const uint16_t*)d_in[3], (const uint16_t*)d_in[4], (const uint16_t*)d_in[5], (const uint16_t*)d_in[6],
    (const int*)d_in[7], (const uint16_t*)d_in[8], (uint16_t*)d_out);
}
```

```cpp
#include <hip/hip_runtime.h>
#include <stdint.h>

#if defined(__HIP_DEVICE_COMPILE__) && __HIP_DEVICE_COMPILE__
#define NOPK __attribute__((target("no-packed-fp32-ops")))
#else
#define NOPK
#endif
#define DI static __device__ __forceinline__ NOPK

typedef __attribute__((ext_vector_type(4))) float f4;
typedef __attribute__((ext_vector_type(2))) float f2;
typedef __attribute__((ext_vector_type(8))) __bf16 b8;
typedef __attribute__((ext_vector_type(2))) __bf16 b2;
typedef __attribute__((ext_vector_type(4))) short s4;
typedef __attribute__((ext_vector_type(4))) unsigned u4;
typedef __attribute__((ext_vector_type(2))) unsigned u2;
typedef __attribute__((ext_vector_type(4))) int i4;
typedef __attribute__((ext_vector_type(6))) unsigned u6;
typedef u4 __attribute__((aligned(2))) u4_a2;
typedef u2 __attribute__((aligned(2))) u2_a2;
typedef i4 __attribute__((aligned(4))) i4_a4;
#define LDS __attribute__((address_space(3)))

constexpr int IMG = 378, NW1 = 54, NWIN = NW1 * NW1, WPB = 12;
constexpr float LOG2E = 1.4426950408889634f;
constexpr float TAU_SAFE = 20.0f;

constexpr int L_W = 0;
constexpr int L_BIAS = L_W + 72 * 1024;
constexpr int L_BIASO = L_BIAS + 36 * 1024;
constexpr int L_BIASQ = L_BIASO + 4 * 1024;
constexpr int L_TAB = L_BIASQ + 6 * 1024;
constexpr int L_ZERO = L_TAB + 3072;
constexpr int L_ODD = L_ZERO + 256;
constexpr int O_K = 0, O_Q = 256, O_O = 512, O_V = 1024, O_SZ = 1792;
constexpr int L_FLAG = L_ODD + WPB * O_SZ;
constexpr int L_TOTAL = L_FLAG + 16;

DI float bf_lo(unsigned u) { return __builtin_bit_cast(float, u << 16); }
DI float bf_hi(unsigned u) { return __builtin_bit_cast(float, u & 0xffff0000u); }
DI unsigned pk(float a, float b) { f2 v = {a, b}; return __builtin_bit_cast(unsigned, __builtin_convertvector(v, b2)); }
DI float bfr(float a) { return bf_lo(pk(a, 0.f)); }
DI f4 mma(u4 a, u4 b, f4 c) { return __builtin_amdgcn_mfma_f32_16x16x32_bf16(__builtin_bit_cast(b8, a), __builtin_bit_cast(b8, b), c, 0, 0, 0); }
DI f4 mma16(u2 a, u2 b, f4 c) { return __builtin_amdgcn_mfma_f32_16x16x16bf16_1k(__builtin_bit_cast(s4, a), __builtin_bit_cast(s4, b), c, 0, 0, 0); }
template <typename T> DI T ldsr(LDS char* base, int off) { return *(LDS T*)(base + off); }
template <typename T> DI void ldsw(LDS char* base, int off, T v) { *(LDS T*)(base + off) = v; }
#define PIN(x) asm volatile("" : "+v"(x))
#define SB() __builtin_amdgcn_sched_barrier(0)
DI f4 first_only(float b) { f2 t = {b, b}; return __builtin_shufflevector(t, t, 0, -1, -1, -1); }
DI LDS char* opaque(LDS char* p) { PIN(p); return p; }
DI int lane_id() { int m = -1; asm volatile("" : "+s"(m)); return __builtin_amdgcn_mbcnt_hi(m, __builtin_amdgcn_mbcnt_lo(m, 0)); }
DI float shx(float v, int lane, int m) { return __builtin_bit_cast(float, __builtin_amdgcn_ds_bpermute((lane ^ m) << 2, __builtin_bit_cast(int, v))); }
DI unsigned tok_off(int wi, int wj, int n) {
    int i = (n * 37) >> 8, j = n - 7 * i;
    int r = wi * 7 + 3 + i, cc = wj * 7 + 3 + j;
    r = r >= IMG ? r - IMG : r; cc = cc >= IMG ? cc - IMG : cc;
    return (unsigned)(r * IMG + cc) * 192u;
}
DI unsigned odd_off(int w0, int lane) {
    int c = lane & 15, w = w0 + (c < WPB ? c : 0), wi = w / NW1;
    return tok_off(wi, w - wi * NW1, 48) + (lane >> 4) * 16;
}
DI void spin_ge(LDS char* lds, int off, int n) {
    while (__hip_atomic_load((LDS int*)(lds + off), __ATOMIC_RELAXED, __HIP_MEMORY_SCOPE_WORKGROUP) < n) __builtin_amdgcn_s_sleep(2);
    __builtin_amdgcn_fence(__ATOMIC_ACQUIRE, "workgroup");
}
DI void post(LDS char* lds, int off, int lane) {
    __builtin_amdgcn_fence(__ATOMIC_RELEASE, "workgroup");
    if (lane == 0) __hip_atomic_fetch_add((LDS int*)(lds + off), 1, __ATOMIC_RELAXED, __HIP_MEMORY_SCOPE_WORKGROUP);
}

template <int NT>
DI void proj_qk(LDS char* wl, int tile0, const u4 (&xf)[NT][3], u2 red, float sce, float sco, bool scale, u6 (&f)[NT]) {
    f4 acc[3][NT];
#pragma unroll
    for (int m = 0; m < 3; ++m)
#pragma unroll
        for (int s = 0; s < 3; ++s) {
            u4 wf = ldsr<u4>(wl, ((tile0 + m) * 3 + s) * 1024);
#pragma unroll
            for (int t = 0; t < NT; ++t) acc[m][t] = mma(wf, xf[t][s], s == 0 ? f4{0.f, 0.f, 0.f, 0.f} : acc[m][t]);
        }
    unsigned pp[NT];
#pragma unroll
    for (int t = 0; t < NT; ++t) {
        float se = __builtin_fmaf(acc[0][t][0], acc[0][t][0], 2.5e-13f), so = __builtin_fmaf(acc[2][t][0], acc[2][t][0], 2.5e-13f);
#pragma unroll
        for (int r = 1; r < 4; ++r) { se = __builtin_fmaf(acc[0][t][r], acc[0][t][r], se); so = __builtin_fmaf(acc[2][t][r], acc[2][t][r], so); }
        se = __builtin_fmaf(acc[1][t][0], acc[1][t][0], se); se = __builtin_fmaf(acc[1][t][1], acc[1][t][1], se);
        so = __builtin_fmaf(acc[1][t][2], acc[1][t][2], so); so = __builtin_fmaf(acc[1][t][3], acc[1][t][3], so);
        pp[t] = pk(se, so);
    }
    f4 tot[(NT + 1) / 2];
#pragma unroll
    for (int u = 0; u < (NT + 1) / 2; ++u) tot[u] = mma16(red, u2{pp[2 * u], pp[2 * u + 1 < NT ? 2 * u + 1 : 2 * u]}, f4{0.f, 0.f, 0.f, 0.f});
#pragma unroll
    for (int t = 0; t < NT; ++t) {
        float re = __builtin_amdgcn_rsqf(tot[t >> 1][(t & 1) * 2]), ro = __builtin_amdgcn_rsqf(tot[t >> 1][(t & 1) * 2 + 1]);
        if (scale) { re *= sce; ro *= sco; }
        f[t] = u6{pk(acc[0][t][0] * re, acc[0][t][1] * re), pk(acc[0][t][2] * re, acc[0][t][3] * re), pk(acc[1][t][0] * re, acc[1][t][1] * re),
                  pk(acc[1][t][2] * ro, acc[1][t][3] * ro), pk(acc[2][t][0] * ro, acc[2][t][1] * ro), pk(acc[2][t][2] * ro, acc[2][t][3] * ro)};
    }
}
DI u4 sub4(u6 f, int hh) { return hh ? __builtin_shufflevector(f, f, 2, 3, 4, 5) : __builtin_shufflevector(f, f, 0, 1, 2, 3); }
DI u4 zer4(u6 f, int hh) { return hh ? u4{0u, f[3], f[4], f[5]} : u4{f[0], f[1], f[2], 0u}; }

template <bool SLOW>
DI void probs(f4 (&S)[3], float s3, const char* mwin, int qt, u4& p0, u4& p1) {
    if (SLOW) {
        const int lane = lane_id();
        const unsigned mo = (qt < 3 ? qt * 16 + (lane & 15) : 48) * 98;
#pragma unroll
        for (int kt = 0; kt < 3; ++kt) {
            u2 mv = *(const u2_a2*)(mwin + (mo + (lane >> 4) * 8) + kt * 32);
            S[kt][0] = __builtin_fmaf(bf_lo(mv[0]), LOG2E, S[kt][0]); S[kt][1] = __builtin_fmaf(bf_hi(mv[0]), LOG2E, S[kt][1]);
            S[kt][2] = __builtin_fmaf(bf_lo(mv[1]), LOG2E, S[kt][2]); S[kt][3] = __builtin_fmaf(bf_hi(mv[1]), LOG2E, S[kt][3]);
        }
        s3 = __builtin_fmaf(bf_lo(*(const unsigned short*)(mwin + mo + 96)), LOG2E, s3);
        float m = s3;
#pragma unroll
        for (int kt = 0; kt < 3; ++kt)
#pragma unroll
            for (int r = 0; r < 4; ++r) m = __builtin_fmaxf(m, S[kt][r]);
        m = __builtin_fmaxf(m, shx(m, lane, 16)); m = __builtin_fmaxf(m, shx(m, lane, 32));
#pragma unroll
        for (int kt = 0; kt < 3; ++kt)
#pragma unroll
            for (int r = 0; r < 4; ++r) S[kt][r] -= m;
        s3 -= m;
    }
#define EX(v) __builtin_amdgcn_exp2f(v)
    p0 = u4{pk(EX(S[0][0]), EX(S[0][1])), pk(EX(S[0][2]), EX(S[0][3])), pk(EX(S[1][0]), EX(S[1][1])), pk(EX(S[1][2]), EX(S[1][3]))};
    p1 = u4{pk(EX(S[2][0]), EX(S[2][1])), pk(EX(S[2][2]), EX(S[2][3])), pk(EX(s3), 0.f), 0u};
#undef EX
}

DI void norm_o(f4 oa, f4 ot, int hh, unsigned& a, unsigned& b, unsigned& t) {
    float rl = __builtin_amdgcn_rcpf(hh ? ot[0] : ot[2]);
    a = pk(oa[0] * rl, oa[1] * rl); b = pk(oa[2] * rl, oa[3] * rl);
    t = hh ? pk(ot[2] * rl, ot[3] * rl) : pk(ot[0] * rl, ot[1] * rl);
}

template <int NT>
DI void out_ln(LDS char* wl, const u4 (&of)[NT][3], u2 red, char* out, const unsigned (&off)[NT], bool valid) {
    f4 acc[6][NT];
    LDS char* wo = opaque(wl + 54 * 1024);
#pragma unroll
    for (int m = 0; m < 6; ++m)
#pragma unroll
        for (int s = 0; s < 3; ++s) {
            u4 wf = ldsr<u4>(wo, (m * 3 + s) * 1024);
#pragma unroll
            for (int t = 0; t < NT; ++t) acc[m][t] = mma(wf, of[t][s], s == 0 ? f4{0.f, 0.f, 0.f, 0.f} : acc[m][t]);
        }
    float ss[4];
#pragma unroll
    for (int t = 0; t < NT; ++t) {
        float a = acc[0][t][0] * acc[0][t][0], b = acc[0][t][1] * acc[0][t][1];
        a = __builtin_fmaf(acc[0][t][2], acc[0][t][2], a); b = __builtin_fmaf(acc[0][t][3], acc[0][t][3], b);
#pragma unroll
        for (int m = 1; m < 6; ++m) {
            a = __builtin_fmaf(acc[m][t][0], acc[m][t][0], a); b = __builtin_fmaf(acc[m][t][1], acc[m][t][1], b);
            a = __builtin_fmaf(acc[m][t][2], acc[m][t][2], a); b = __builtin_fmaf(acc[m][t][3], acc[m][t][3], b);
        }
        ss[t] = a + b;
    }
#pragma unroll
    for (int t = NT; t < 4; ++t) ss[t] = 0.f;
    f4 tot = mma16(red, u2{pk(ss[0], ss[1]), pk(ss[2], ss[3])}, f4{0.f, 0.f, 0.f, 0.f});
#pragma unroll
    for (int t = 0; t < NT; ++t) {
        float rs = __builtin_amdgcn_rsqf(__builtin_fmaf(tot[t], 1.0f / 96.0f, 1e-5f));
#pragma unroll
        for (int u = 0; u < 3; ++u) {
            u4 o = u4{pk(acc[2 * u][t][0] * rs, acc[2 * u][t][1] * rs), pk(acc[2 * u][t][2] * rs, acc[2 * u][t][3] * rs),
                      pk(acc[2 * u + 1][t][0] * rs, acc[2 * u + 1][t][1] * rs), pk(acc[2 * u + 1][t][2] * rs, acc[2 * u + 1][t][3] * rs)};
            if (valid) *(u4*)(out + off[t] + u * 64) = o;
        }
    }
}

template <bool SLOW>
DI void window_body(LDS char* lds, LDS char* wl, const u4 (&xf)[3][3], u2 red, const float (&tau)[4],
                    const char* mwin, char* out, int wi, int wj, int v) {
    const int lane = lane_id();
    LDS char* ll = opaque(lds + lane * 16 + L_BIAS);
    LDS char* lk = opaque(lds + L_ODD + v * O_SZ + (lane >> 4) * 16);
    u4 of[3][3];
#pragma unroll
    for (int p = 0; p < 2; ++p) {
        u6 qf[3], kq[3]; u4 kf[2][3], va[2][2], vt[2][2];
        SB();
        proj_qk<3>(wl, p * 3, xf, red, tau[2 * p], tau[2 * p + 1], true, qf);
#pragma unroll
        for (int t = 0; t < 3; ++t) PIN(qf[t]);
        SB();
        proj_qk<3>(wl, 6 + p * 3, xf, red, 1.f, 1.f, false, kq);
#pragma unroll
        for (int t = 0; t < 3; ++t) { kf[0][t] = zer4(kq[t], 0); kf[1][t] = zer4(kq[t], 1); PIN(kf[0][t]); PIN(kf[1][t]); }
        SB();
        if (p == 0) spin_ge(lds, L_FLAG, WPB);
        const int c = lane_id() & 15;
        {
            unsigned sel = (c & 2) ? 0xffffffffu : 0u, ones = 0x3f803f80u;
            PIN(sel); PIN(ones);
            LDS char* lv = opaque(lds + L_ODD + O_V + v * O_SZ + c * 8);
#pragma unroll
            for (int m = 0; m < 3; ++m) {
                f4 acc[3];
#pragma unroll
                for (int s = 0; s < 3; ++s) {
                    u4 wf = ldsr<u4>(wl, ((12 + p * 3 + m) * 3 + s) * 1024);
#pragma unroll
                    for (int t = 0; t < 3; ++t) acc[t] = mma(xf[t][s], wf, s == 0 ? f4{0.f, 0.f, 0.f, 0.f} : acc[t]);
                }
                u2 vo = ldsr<u2>(lv, (p * 3 + m) * 128);
                u4 f0 = u4{pk(acc[0][0], acc[0][1]), pk(acc[0][2], acc[0][3]), pk(acc[1][0], acc[1][1]), pk(acc[1][2], acc[1][3])};
                u4 f1 = u4{pk(acc[2][0], acc[2][1]), pk(acc[2][2], acc[2][3]), vo[0], vo[1]};
                if (m == 1) {
#pragma unroll
                    for (int i = 0; i < 4; ++i) {
                        vt[0][0][i] = __builtin_amdgcn_bitop3_b32(sel, ones, f0[i], 0xca); vt[1][0][i] = __builtin_amdgcn_bitop3_b32(sel, f0[i], ones, 0xca);
                        vt[0][1][i] = __builtin_amdgcn_bitop3_b32(sel, ones, f1[i], 0xca); vt[1][1][i] = __builtin_amdgcn_bitop3_b32(sel, f1[i], ones, 0xca);
                    }
                } else { va[m >> 1][0] = f0; va[m >> 1][1] = f1; }
            }
        }
#pragma unroll
        for (int i = 0; i < 2; ++i) { PIN(va[0][i]); PIN(va[1][i]); PIN(vt[0][i]); PIN(vt[1][i]); }
        SB();
        {
            f4 S[3], S3;
#pragma unroll
            for (int kt = 0; kt < 3; ++kt) S[kt] = ldsr<f4>(ll, L_BIASQ - L_BIAS + (p * 3 + kt) * 1024);
            S3 = first_only(ldsr<float>(ll, L_BIASO - L_BIAS + p * 2048 + 12));
#pragma unroll
            for (int hh = 0; hh < 2; ++hh) {
                u4 qo = ldsr<u4>(c == hh ? lk : lds + (L_ZERO - O_Q), O_Q + (2 * p + hh) * 64);
#pragma unroll
                for (int kt = 0; kt < 3; ++kt) S[kt] = mma(kf[hh][kt], qo, S[kt]);
                S3 = mma(ldsr<u4>(lk, O_K + (2 * p + hh) * 64), qo, S3);
            }
            u4 p0, p1;
            probs<SLOW>(S, S3[0], mwin, 3, p0, p1);
#pragma unroll
            for (int hh = 0; hh < 2; ++hh) {
                f4 oa = mma(va[hh][1], p1, mma(va[hh][0], p0, f4{0.f, 0.f, 0.f, 0.f}));
                f4 ot = mma(vt[hh][1], p1, mma(vt[hh][0], p0, f4{0.f, 0.f, 0.f, 0.f}));
                if (c == hh) {
                    ldsw<f4>(lk, O_O + (2 * p + hh) * 128, oa);
                    ldsw<f4>(lk, O_O + (2 * p + hh) * 128 + 64, ot);
                }
            }
            if (p == 1) post(lds, L_FLAG + 4, lane_id());
        }
#pragma unroll
        for (int hh = 0; hh < 2; ++hh) {
            SB();
            u4 ko = ldsr<u4>(lk, O_K + (2 * p + hh) * 64);
#pragma unroll
            for (int qt = 0; qt < 3; ++qt) {
                const int h = 2 * p + hh;
                f4 S[3], S3;
                const u4 q = sub4(qf[qt], hh);
#pragma unroll
                for (int kt = 0; kt < 3; ++kt) S[kt] = mma(kf[hh][kt], q, ldsr<f4>(ll, ((h * 3 + qt) * 3 + kt) * 1024));
                S3 = mma(ko, q, first_only(ldsr<float>(ll, L_BIASO - L_BIAS + h * 1024 + qt * 4)));
                u4 p0, p1;
                probs<SLOW>(S, S3[0], mwin, qt, p0, p1);
                f4 oa = mma(va[hh][1], p1, mma(va[hh][0], p0, f4{0.f, 0.f, 0.f, 0.f}));
                f4 ot = mma(vt[hh][1], p1, mma(vt[hh][0], p0, f4{0.f, 0.f, 0.f, 0.f}));
                unsigned a, b, t;
                norm_o(oa, ot, hh, a, b, t);
                PIN(a); PIN(b); PIN(t);
                of[qt][p][2 * hh] = a; of[qt][p][2 * hh + 1] = b; of[qt][2][h] = t;
                SB();
            }
        }
    }
    unsigned xoff[3];
    const int l2 = lane_id();
#pragma unroll
    for (int t = 0; t < 3; ++t) xoff[t] = tok_off(wi, wj, t * 16 + (l2 & 15)) + (l2 >> 4) * 16;
    out_ln<3>(wl, of, red, out, xoff, true);
}

__global__ __launch_bounds__(WPB * 64) NOPK void swin_kernel(const char* __restrict__ x, const char* __restrict__ wqkv, const char* __restrict__ wo,
        const char* __restrict__ lscale, const char* __restrict__ w1, const char* __restrict__ w2, const char* __restrict__ rc,
        const int* __restrict__ ridx, const char* __restrict__ mask, char* __restrict__ out) {
    extern __shared__ __attribute__((aligned(16))) char smem[];
    LDS char* lds = (LDS char*)smem;
    const int tid = __builtin_amdgcn_workitem_id_x(), lane = tid & 63, c = lane & 15, g = lane >> 4;
    const int v = __builtin_amdgcn_readfirstlane(tid >> 6);
    const int w0 = __builtin_amdgcn_workgroup_id_x() * WPB, w = w0 + v;
    const int wi = w / NW1, wj = w - wi * NW1;

    const int e = (tid >> 2) < 169 ? (tid >> 2) : 168, jq = tid & 3;
    const unsigned rcv = *(const unsigned*)(rc + e * 4);
    const u2 w1a = *(const u2*)(w1 + jq * 8), w1b = *(const u2*)(w1 + 32 + jq * 8);
    const u4 w2a = *(const u4*)(w2 + jq * 32), w2b = *(const u4*)(w2 + jq * 32 + 16);
    const unsigned lsv = *(const unsigned short*)(lscale + jq * 2);
    u4 wr[8];
    const int wq = tid >> 2, ws = v < 7 ? wq / 36 : v - 7, wc = v < 7 ? wq - ws * 36 : (lane >> 2);
    const bool wact = v < 7 ? tid < 432 : (v < 10 && lane < 48);
    if (wact) {
        if (v < 7) {
#pragma unroll
            for (int j = 0; j < 8; ++j) wr[j] = *(const u4*)(wqkv + (ws * 32 + jq * 8 + j) * 576 + wc * 16);
        } else {
#pragma unroll
            for (int j = 0; j < 8; ++j) {
                int ch = ws < 2 ? 24 * (2 * ws + (j >> 2)) + 4 * jq + (j & 3) : 24 * (j >> 1) + 16 + 2 * jq + (j & 1);
                wr[j] = *(const u4*)(wo + ch * 192 + wc * 16);
            }
        }
    } else {
#pragma unroll
        for (int j = 0; j < 8; ++j) wr[j] = u4{0u, 0u, 0u, 0u};
    }
    i4 ix; int ixo = 0; u4 xo[1][3];
    if (v < 9) {
        int qt = v / 3, kt = v - qt * 3;
        ix = *(const i4_a4*)(ridx + (qt * 16 + c) * 49 + kt * 16 + g * 4);
    } else {
        ix = *(const i4_a4*)(ridx + 48 * 49 + (v - 9) * 16 + g * 4);
        ixo = ridx[(g == 0 ? (v - 9) * 16 + c : 48) * 49 + 48];
        const unsigned ooff = odd_off(w0, lane);
#pragma unroll
        for (int s = 0; s < 3; ++s) xo[0][s] = *(const u4*)(x + ooff + s * 64);
    }
    u4 xf[3][3]; unsigned xoff[3];
#pragma unroll
    for (int t = 0; t < 3; ++t) {
        xoff[t] = tok_off(wi, wj, t * 16 + c) + g * 16;
#pragma unroll
        for (int s = 0; s < 3; ++s) xf[t][s] = *(const u4*)(x + xoff[t] + s * 64);
    }
    const char* mwin = mask + (size_t)w * 4802;
    u4 mk[5];
#pragma unroll
    for (int i = 0; i < 5; ++i) { int o = (i * 64 + lane) * 16; mk[i] = *(const u4_a2*)(mwin + (o < 4786 ? o : 4786)); }

    float tau[4]; bool slow;
    {
        float r0 = bf_lo(rcv), r1 = bf_hi(rcv), hv[4], z[4];
        hv[0] = bfr(__builtin_fmaf(r1, bf_lo(w1b[0]), r0 * bf_lo(w1a[0]))); hv[1] = bfr(__builtin_fmaf(r1, bf_hi(w1b[0]), r0 * bf_hi(w1a[0])));
        hv[2] = bfr(__builtin_fmaf(r1, bf_lo(w1b[1]), r0 * bf_lo(w1a[1]))); hv[3] = bfr(__builtin_fmaf(r1, bf_hi(w1b[1]), r0 * bf_hi(w1a[1])));
#pragma unroll
        for (int j = 0; j < 4; ++j) hv[j] = hv[j] > 0.f ? hv[j] : hv[j] * 0.f;
        z[0] = hv[0] * bf_lo(w2a[0]) + hv[1] * bf_lo(w2a[2]) + hv[2] * bf_lo(w2b[0]) + hv[3] * bf_lo(w2b[2]);
        z[1] = hv[0] * bf_hi(w2a[0]) + hv[1] * bf_hi(w2a[2]) + hv[2] * bf_hi(w2b[0]) + hv[3] * bf_hi(w2b[2]);
        z[2] = hv[0] * bf_lo(w2a[1]) + hv[1] * bf_lo(w2a[3]) + hv[2] * bf_lo(w2b[1]) + hv[3] * bf_lo(w2b[3]);
        z[3] = hv[0] * bf_hi(w2a[1]) + hv[1] * bf_hi(w2a[3]) + hv[2] * bf_hi(w2b[1]) + hv[3] * bf_hi(w2b[3]);
#pragma unroll
        for (int h = 0; h < 4; ++h) {
            z[h] += __builtin_bit_cast(float, __builtin_amdgcn_update_dpp(0, __builtin_bit_cast(int, z[h]), 0xB1, 0xf, 0xf, true));
            z[h] += __builtin_bit_cast(float, __builtin_amdgcn_update_dpp(0, __builtin_bit_cast(int, z[h]), 0x4E, 0xf, 0xf, true));
        }
        float zz = bfr(jq == 0 ? z[0] : jq == 1 ? z[1] : jq == 2 ? z[2] : z[3]);
        float sg = bfr(__builtin_amdgcn_rcpf(1.0f + __builtin_amdgcn_exp2f(-zz * LOG2E)));
        float lc = bfr(__builtin_fminf(__builtin_fmaxf(bf_lo(lsv), -100.0f), 4.6052f));
        float tv = bfr(__builtin_amdgcn_exp2f(lc * LOG2E));
        ldsw<float>(lds, L_TAB + tid * 4, (16.0f * sg - 16.0f - tv) * LOG2E);
        float tl = tv * LOG2E; PIN(tl);
#pragma unroll
        for (int h = 0; h < 4; ++h) tau[h] = __builtin_bit_cast(float, __builtin_amdgcn_readlane(__builtin_bit_cast(int, tl), h));
        slow = __builtin_amdgcn_ballot_w64(!(tv <= TAU_SAFE)) != 0;
    }
    if (tid < 64) ldsw<unsigned>(lds, L_ZERO + tid * 4, 0u);
    if (tid < 2) ldsw<int>(lds, L_FLAG + tid * 4, 0);

    if (v < 7 && wact) {
        int T = wc / 12, rem = wc - T * 12, h = rem / 3, part = rem - h * 3;
        int tile = T * 6 + (h >> 1) * 3 + (part < 2 ? (h & 1) * 2 : 1);
        int base = (tile * 3 + ws) * 1024 + jq * 256;
#pragma unroll
        for (int i = 0; i < 8; ++i) {
            int rho = part < 2 ? 8 * part + i : 4 * (i >> 1) + (i & 1) + 2 * (h & 1);
            const unsigned ps = (i & 1) ? 0x07060302u : 0x05040100u;
            u4 f = u4{__builtin_amdgcn_perm(wr[1][i >> 1], wr[0][i >> 1], ps), __builtin_amdgcn_perm(wr[3][i >> 1], wr[2][i >> 1], ps),
                      __builtin_amdgcn_perm(wr[5][i >> 1], wr[4][i >> 1], ps), __builtin_amdgcn_perm(wr[7][i >> 1], wr[6][i >> 1], ps)};
            ldsw<u4>(lds, L_W + base + ((rho ^ jq) << 4), f);
        }
    }
    __builtin_amdgcn_fence(__ATOMIC_RELEASE, "workgroup");
    __builtin_amdgcn_s_barrier();
    __builtin_amdgcn_fence(__ATOMIC_ACQUIRE, "workgroup");

    LDS char* wl = lds + L_W + g * 256 + ((c ^ g) << 4);
    const u2 red = u2{(c & 3) == 0 ? 0x3f80u : (c & 3) == 1 ? 0x3f800000u : 0u, (c & 3) == 2 ? 0x3f80u : (c & 3) == 3 ? 0x3f800000u : 0u};
    if (v < 9) {
        f4 t0 = ldsr<f4>(lds, L_TAB + ix[0] * 16), t1 = ldsr<f4>(lds, L_TAB + ix[1] * 16), t2 = ldsr<f4>(lds, L_TAB + ix[2] * 16), t3 = ldsr<f4>(lds, L_TAB + ix[3] * 16);
#pragma unroll
        for (int h = 0; h < 4; ++h) ldsw<f4>(lds, L_BIAS + (h * 9 + v) * 1024 + lane * 16, f4{t0[h], t1[h], t2[h], t3[h]});
    } else {
        const float ninf = -__builtin_inff();
        f4 to = ldsr<f4>(lds, L_TAB + ixo * 16);
#pragma unroll
        for (int h = 0; h < 4; ++h) ldsw<float>(lds, L_BIASO + h * 1024 + lane * 16 + (v - 9) * 4, g == 0 ? to[h] : ninf);
#pragma unroll
        for (int p = 0; p < 2; ++p) {
            int hc = 2 * p + (c & 1);
            f4 b;
#pragma unroll
            for (int r = 0; r < 4; ++r) b[r] = ldsr<float>(lds, L_TAB + ix[r] * 16 + hc * 4);
            ldsw<f4>(lds, L_BIASQ + (p * 3 + v - 9) * 1024 + lane * 16, b);
            if (v == 9) ldsw<float>(lds, L_BIASO + p * 2048 + (lane ^ (g < 2 ? 16 : 0)) * 16 + 12, g == 1 ? (c & 1 ? to[2 * p + 1] : to[2 * p]) : ninf);
        }
        if (v < 11) {
#pragma unroll
            for (int p = 0; p < 2; ++p) {
                u6 f[1];
                proj_qk<1>(wl + (v - 9) * 18 * 1024, p * 3, xo, red, v == 9 ? tau[2 * p] : 1.f, v == 9 ? tau[2 * p + 1] : 1.f, true, f);
                if (c < WPB) {
                    int o = L_ODD + (v == 9 ? O_Q : O_K) + c * O_SZ + g * 16 + p * 128;
                    ldsw<u4>(lds, o, zer4(f[0], 0)); ldsw<u4>(lds, o + 64, zer4(f[0], 1));
                }
            }
        } else {
#pragma unroll
            for (int m = 0; m < 6; ++m) {
                f4 a = f4{0.f, 0.f, 0.f, 0.f};
#pragma unroll
                for (int s = 0; s < 3; ++s) a = mma(ldsr<u4>(wl, ((12 + m) * 3 + s) * 1024), xo[0][s], a);
                if (c < WPB) {
                    int o = L_ODD + O_V + c * O_SZ + m * 128 + g * 32;
                    ldsw<u4>(lds, o, u4{pk(a[0], 0.f), 0u, pk(a[1], 0.f), 0u});
                    ldsw<u4>(lds, o + 16, u4{pk(a[2], 0.f), 0u, pk(a[3], 0.f), 0u});
                }
            }
        }
    }
    if (v >= 7 && v < 10) {
        float mean[8];
#pragma unroll
        for (int j = 0; j < 8; ++j) {
            float s = 0.f;
#pragma unroll
            for (int i = 0; i < 4; ++i) s += bf_lo(wr[j][i]) + bf_hi(wr[j][i]);
            s += shx(s, lane, 4); s += shx(s, lane, 8); s += shx(s, lane, 16); s += shx(s, lane, 32);
            mean[j] = s * (1.0f / 96.0f);
        }
        if (wact) {
#pragma unroll
            for (int i = 0; i < 8; ++i) {
                u4 f;
#pragma unroll
                for (int j = 0; j < 4; ++j) {
                    unsigned a = wr[2 * j][i >> 1], b = wr[2 * j + 1][i >> 1];
                    f[j] = pk(((i & 1) ? bf_hi(a) : bf_lo(a)) - mean[2 * j], ((i & 1) ? bf_hi(b) : bf_lo(b)) - mean[2 * j + 1]);
                }
                int m = 2 * (wc >> 2) + (i >> 2), rho = 4 * (wc & 3) + (i & 3);
                ldsw<u4>(lds, L_W + (54 + m * 3 + ws) * 1024 + jq * 256 + ((rho ^ jq) << 4), f);
            }
        }
    }
    post(lds, L_FLAG, lane);

    unsigned mo = 0;
#pragma unroll
    for (int i = 0; i < 5; ++i) mo |= mk[i][0] | mk[i][1] | mk[i][2] | mk[i][3];
    slow = slow || __builtin_amdgcn_ballot_w64((mo & 0x7fff7fffu) != 0) != 0;
    if (slow) window_body<true>(lds, wl, xf, red, tau, mwin, out, wi, wj, v);
    else window_body<false>(lds, wl, xf, red, tau, mwin, out, wi, wj, v);

    if (v == 0) {
        spin_ge(lds, L_FLAG + 4, WPB);
        const int l2 = lane_id();
        const int c2 = l2 & 15;
        u4 of[1][3];
        LDS char* ol = lds + L_ODD + O_O + (c2 < WPB ? c2 : 0) * O_SZ + (l2 >> 4) * 16;
#pragma unroll
        for (int h = 0; h < 4; ++h) {
            unsigned a, b, t;
            norm_o(ldsr<f4>(ol, h * 128), ldsr<f4>(ol, h * 128 + 64), h & 1, a, b, t);
            of[0][h >> 1][2 * (h & 1)] = a; of[0][h >> 1][2 * (h & 1) + 1] = b; of[0][2][h] = t;
        }
        const unsigned ooff[1] = {odd_off(w0, l2)};
        out_ln<1>(wl, of, red, out, ooff, c2 < WPB);
    }
}

extern "C" void kernel_launch(void* const* d_in, const int* in_sizes, int n_in, void* d_out, int out_size, void* d_ws, size_t ws_size,
                              hipStream_t stream) {
    (void)in_sizes; (void)n_in; (void)out_size; (void)d_ws; (void)ws_size;
    (void)hipFuncSetAttribute((const void*)swin_kernel, hipFuncAttributeMaxDynamicSharedMemorySize, L_TOTAL);
    swin_kernel<<<NWIN / WPB, WPB * 64, L_TOTAL, stream>>>((const char*)d_in[0], (const char*)d_in[1], (const char*)d_in[2], (const char*)d_in[3],
        (const char*)d_in[4], (const char*)d_in[5], (const char*)d_in[6], (const int*)d_in[7], (const char*)d_in[8], (char*)d_out);
}
```

```cpp
#include <hip/hip_runtime.h>

constexpr int WPB = 12, NBLK = 253, L_TOTAL = 162880 + 48;

__global__ __launch_bounds__(WPB * 64) void swin_kernel(const void* x, const void* wqkv, const void* wo, const void* lscale, const void* w1,
                                                        const void* w2, const void* rc, const void* ridx, const void* mask, void* out) {
    unsigned tid = __builtin_amdgcn_workitem_id_x();
    const int v = __builtin_amdgcn_readfirstlane(tid >> 6), blk = __builtin_amdgcn_workgroup_id_x();
    asm volatile(
    ".Ltop_%=:\n"
    "s_load_dwordx16 s[64:79], %[ka], 0x0\n"
    "s_load_dwordx4 s[92:95], %[ka], 0x40\n"
    "s_lshr_b32 s50, %[v], 2\n"
    "s_cmp_eq_u32 s50, 0\n"
    "s_cbranch_scc1 .Lra_%=\n"
    "s_cmp_eq_u32 s50, 2\n"
    "s_cbranch_scc1 .Lrc_%=\n"
    "s_setprio 2\n"
    "v_bfe_u32 v124, v40, 4, 2\n"
    "v_bfe_u32 v91, v40, 2, 2\n"
    "v_and_b32 v1, 3, v40\n"
    "s_sub_i32 s50, %[v], 4\n"
    "s_movk_i32 s62, 4608\n"
    "s_lshl_b32 s51, s50, 2\n"
    "v_add_u32 v98, s51, v124\n"
    "v_mul_u32_u24 v15, 57, v98\n"
    "v_lshrrev_b32 v15, 9, v15\n"
    "v_mad_i32_i24 v8, v15, -9, v98\n"
    "v_lshl_add_u32 v8, v8, 2, v1\n"
    "v_lshl_add_u32 v35, v15, 2, v91\n"
    "v_mul_u32_u24 v35, s62, v35\n"
    "v_lshl_add_u32 v35, v8, 4, v35\n"
    "s_mul_i32 s51, s50, 3\n"
    "s_add_i32 s51, s51, 16\n"
    "v_add_u32 v19, s51, v124\n"
    "s_mov_b32 s60, 0\n"
    "s_mov_b32 s61, 0xffff0000\n"
    "v_cndmask_b32 v19, v19, 27, s[60:61]\n"
    "v_mul_u32_u24 v53, 57, v19\n"
    "v_lshrrev_b32 v53, 9, v53\n"
    "v_mad_i32_i24 v93, v53, -9, v19\n"
    "v_lshl_add_u32 v93, v93, 2, v1\n"
    "v_lshl_add_u32 v38, v53, 2, v91\n"
    "v_mul_u32_u24 v38, s62, v38\n"
    "v_lshl_add_u32 v38, v93, 4, v38\n"
    "s_waitcnt lgkmcnt(0)\n"
    "s_mov_b32 s52, s66\n"
    "s_and_b32 s53, s67, 0xffff\n"
    "s_mov_b32 s54, 55296\n"
    "s_mov_b32 s55, 0x20000\n"
    "s_load_dwordx2 s[70:71], s[70:71], 0x0\n"
    "buffer_load_dwordx4 v[124:127], v35, s[52:55], 0 offen offset:0\n"
    "v_mul_u32_u24 v130, 43, v8\n"
    "v_lshrrev_b32 v130, 7, v130\n"
    "v_mad_i32_i24 v59, v130, -3, v8\n"
    "v_cmp_eq_u32 s[60:61], 2, v59\n"
    "v_and_b32 v110, 1, v130\n"
    "v_lshrrev_b32 v130, 1, v130\n"
    "v_lshlrev_b32 v110, 1, v110\n"
    "v_cndmask_b32 v90, v110, 1, s[60:61]\n"
    "buffer_load_dwordx4 v[136:139], v35, s[52:55], 0 offen offset:576\n"
    "v_mad_u32_u24 v90, v130, 3, v90\n"
    "v_mad_u32_u24 v90, v90, 3, v15\n"
    "v_lshl_add_u32 v90, v90, 2, v91\n"
    "v_mul_u32_u24 v59, 12, v59\n"
    "v_cndmask_b32 v59, v59, v110, s[60:61]\n"
    "v_xor_b32 v59, v59, v91\n"
    "v_lshl_add_u32 v59, v90, 4, v59\n"
    "v_lshlrev_b32 v120, 4, v59\n"
    "buffer_load_dwordx4 v[140:143], v35, s[52:55], 0 offen offset:1152\n"
    "v_cndmask_b32 v116, 32, 64, s[60:61]\n"
    "v_add_lshl_u32 v28, v116, -16, 2\n"
    "v_xor_b32 v115, v120, v116\n"
    "v_xor_b32 v2, v120, v28\n"
    "v_xor_b32 v57, v115, v28\n"
    "v_xor_b32 v27, 16, v120\n"
    "v_xor_b32 v64, 16, v115\n"
    "v_xor_b32 v14, 16, v2\n"
    "buffer_load_dwordx4 v[20:23], v35, s[52:55], 0 offen offset:1728\n"
    "v_xor_b32 v121, 16, v57\n"
    "v_mul_u32_u24 v37, 43, v93\n"
    "v_lshrrev_b32 v37, 7, v37\n"
    "v_mad_i32_i24 v122, v37, -3, v93\n"
    "v_cmp_eq_u32 s[60:61], 2, v122\n"
    "v_and_b32 v112, 1, v37\n"
    "v_lshrrev_b32 v37, 1, v37\n"
    "v_lshlrev_b32 v112, 1, v112\n"
    "buffer_load_dwordx4 v[28:31], v35, s[52:55], 0 offen offset:2304\n"
    "v_cndmask_b32 v42, v112, 1, s[60:61]\n"
    "v_mad_u32_u24 v42, v37, 3, v42\n"
    "v_mad_u32_u24 v42, v42, 3, v53\n"
    "v_lshl_add_u32 v42, v42, 2, v91\n"
    "v_mul_u32_u24 v122, 12, v122\n"
    "v_cndmask_b32 v122, v122, v112, s[60:61]\n"
    "v_xor_b32 v122, v122, v91\n"
    "v_lshl_add_u32 v122, v42, 4, v122\n"
    "buffer_load_dwordx4 v[80:83], v35, s[52:55], 0 offen offset:2880\n"
    "v_lshlrev_b32 v117, 4, v122\n"
    "v_cndmask_b32 v84, 32, 64, s[60:61]\n"
    "v_add_lshl_u32 v154, v84, -16, 2\n"
    "v_xor_b32 v26, v117, v84\n"
    "v_xor_b32 v159, v117, v154\n"
    "v_xor_b32 v17, v26, v154\n"
    "v_xor_b32 v112, 16, v117\n"
    "v_xor_b32 v101, 16, v26\n"
    "buffer_load_dwordx4 v[88:91], v35, s[52:55], 0 offen offset:3456\n"
    "v_xor_b32 v74, 16, v159\n"
    "v_xor_b32 v118, 16, v17\n"
    "s_mov_b64 s[96:97], s[64:65]\n"
    "s_mov_b64 s[98:99], s[68:69]\n"
    "s_lshl_b32 s50, %[blk], 2\n"
    "s_add_i32 s50, s50, %[v]\n"
    "s_mul_i32 s51, %[blk], 12\n"
    "s_add_i32 s51, s51, %[v]\n"
    "buffer_load_dwordx4 v[44:47], v35, s[52:55], 0 offen offset:4032\n"
    "s_sub_i32 s51, s51, 212\n"
    "s_cmp_lt_u32 %[blk], 27\n"
    "s_cselect_b32 s51, s50, s51\n"
    "s_cselect_b32 s60, 8, 12\n"
    "s_cselect_b32 s61, 4, 0\n"
    "s_sub_i32 s51, s51, 4\n"
    "s_mul_i32 s29, s51, 4947\n"
    "s_lshr_b32 s29, s29, 18\n"
    "buffer_load_dwordx4 v[148:151], v38, s[52:55], 0 offen offset:0\n"
    "s_mul_i32 s30, s29, 53\n"
    "s_sub_i32 s30, s51, s30\n"
    "s_sub_i32 s38, s50, 54\n"
    "s_min_u32 s38, s38, 53\n"
    "s_min_u32 s39, s50, 53\n"
    "s_add_i32 s50, s50, 2702\n"
    "s_lshr_b32 s31, %[v], 3\n"
    "s_cmp_lt_u32 %[v], s61\n"
    "buffer_load_dwordx4 v[32:35], v38, s[52:55], 0 offen offset:576\n"
    "s_cselect_b32 s29, s38, s29\n"
    "s_cselect_b32 s30, s39, s30\n"
    "s_cselect_b32 s51, s50, s51\n"
    "s_cselect_b32 s31, 2, s31\n"
    "s_cmp_lt_u32 %[v], s60\n"
    "s_cselect_b32 s51, s51, 9999\n"
    "s_cmp_lt_u32 s51, 2809\n"
    "s_cselect_b32 s31, s31, 3\n"
    "buffer_load_dwordx4 v[164:167], v38, s[52:55], 0 offen offset:1152\n"
    "s_mul_i32 s28, s29, 2646\n"
    "s_mul_i32 s50, s30, 7\n"
    "s_add_i32 s28, s28, s50\n"
    "s_add_i32 s28, s28, 1137\n"
    "s_mul_i32 s28, s28, 192\n"
    "s_add_u32 s24, s64, s28\n"
    "s_addc_u32 s25, s65, 0\n"
    "s_and_b32 s25, s25, 0xffff\n"
    "buffer_load_dwordx4 v[92:95], v38, s[52:55], 0 offen offset:1728\n"
    "s_sub_i32 s18, 27433728, s28\n"
    "s_cmp_lt_u32 s31, 2\n"
    "s_cselect_b32 s26, s18, 0\n"
    "s_mov_b32 s27, 0x20000\n"
    "v_and_b32 v37, 63, v40\n"
    "v_and_b32 v53, 15, v40\n"
    "v_bfe_u32 v75, v40, 4, 2\n"
    "v_and_b32 v67, 48, v40\n"
    "buffer_load_dwordx4 v[84:87], v38, s[52:55], 0 offen offset:2304\n"
    "s_movk_i32 s50, 371\n"
    "s_movk_i32 s51, 192\n"
    "v_add_u32 v157, 0, v53\n"
    "v_mul_u32_u24 v3, 37, v157\n"
    "v_lshrrev_b32 v3, 8, v3\n"
    "v_mad_u32_u24 v3, v3, s50, v157\n"
    "v_mad_u32_u24 v13, v3, s51, v67\n"
    "v_add_u32 v79, 16, v53\n"
    "buffer_load_dwordx4 v[160:163], v38, s[52:55], 0 offen offset:2880\n"
    "v_mul_u32_u24 v71, 37, v79\n"
    "v_lshrrev_b32 v71, 8, v71\n"
    "v_mad_u32_u24 v71, v71, s50, v79\n"
    "v_mad_u32_u24 v73, v71, s51, v67\n"
    "v_add_u32 v103, 32, v53\n"
    "v_mul_u32_u24 v5, 37, v103\n"
    "v_lshrrev_b32 v5, 8, v5\n"
    "v_mad_u32_u24 v5, v5, s50, v103\n"
    "buffer_load_dwordx4 v[104:107], v38, s[52:55], 0 offen offset:3456\n"
    "v_mad_u32_u24 v128, v5, s51, v67\n"
    "s_add_u32 s16, s94, s28\n"
    "s_addc_u32 s17, s95, 0\n"
    "s_and_b32 s17, s17, 0xffff\n"
    "s_mov_b32 s19, 0x20000\n"
    "s_mul_i32 s50, s29, 54\n"
    "s_add_i32 s50, s50, s30\n"
    "s_mul_i32 s50, s50, 4802\n"
    "buffer_load_dwordx4 v[108:111], v38, s[52:55], 0 offen offset:4032\n"
    "s_barrier\n"
    "s_setprio 3\n"
    "s_add_u32 s38, s92, s50\n"
    "s_addc_u32 s39, s93, 0\n"
    "s_sub_u32 s20, s38, 57344\n"
    "s_subb_u32 s21, s39, 0\n"
    "s_and_b32 s21, s21, 0xffff\n"
    "s_mov_b32 s22, 62146\n"
    "s_mov_b32 s23, 0x20000\n"
    "s_cmp_eq_u32 s29, 53\n"
    "s_cselect_b32 s29, 4, 99\n"
    "s_cmp_eq_u32 s30, 53\n"
    "s_cselect_b32 s30, 4, 99\n"
    "s_and_b32 s34, %[v], 3\n"
    "s_mul_i32 s34, s34, 4816\n"
    "s_add_i32 s34, s34, 143616\n"
    "s_movk_i32 s37, 4096\n"
    "s_mov_b32 s40, 0x2a0cbccc\n"
    "s_mov_b32 s41, 0x2a0cbccc\n"
    "s_mov_b32 s42, 0x387ba882\n"
    "s_mov_b32 s43, 0x387ba882\n"
    "v_lshrrev_b32 v134, 1, v53\n"
    "v_and_b32 v134, 4, v134\n"
    "v_xor_b32 v134, v134, v53\n"
    "v_xor_b32 v134, v134, v75\n"
    "v_add_u32 v134, v134, v67\n"
    "v_lshlrev_b32 v36, 4, v134\n"
    "v_lshlrev_b32 v37, 4, v37\n"
    "v_add_u32 v37, 0xe000, v37\n"
    "s_mul_i32 s50, %[v], 1472\n"
    "s_add_i32 s50, s50, 125952\n"
    "v_add_u32 v38, s50, v67\n"
    "s_mov_b32 s68, 0\n"
    "s_mov_b32 s69, -1\n"
    "s_waitcnt lgkmcnt(0)\n"
    "v_mov_b32 v66, s70\n"
    "v_mov_b32 v70, s71\n"
    "v_cndmask_b32 v66, v66, v70, s[68:69]\n"
    "s_mov_b32 s68, 0xffff0000\n"
    "s_mov_b32 s69, 0xffff0000\n"
    "v_lshlrev_b32 v70, 16, v66\n"
    "v_and_b32 v66, 0xffff0000, v66\n"
    "v_cndmask_b32 v66, v70, v66, s[68:69]\n"
    "v_max_f32 v66, 0xc2c80000, v66\n"
    "v_min_f32 v66, 0x40935dcc, v66\n"
    "v_cvt_pk_bf16_f32 v66, v66, 0\n"
    "v_lshlrev_b32 v66, 16, v66\n"
    "v_mul_f32 v66, 0x3fb8aa3b, v66\n"
    "v_exp_f32 v66, v66\n"
    "s_nop 0\n"
    "v_cvt_pk_bf16_f32 v66, v66, 0\n"
    "v_lshlrev_b32 v66, 16, v66\n"
    "v_mul_f32 v3, 0x3fb8aa3b, v66\n"
    "s_mov_b32 s50, 0x41e6d4ca\n"
    "s_nop 0\n"
    "v_readlane_b32 s44, v3, 0\n"
    "v_readlane_b32 s45, v3, 16\n"
    "v_readlane_b32 s46, v3, 32\n"
    "v_readlane_b32 s47, v3, 48\n"
    "v_cmp_nge_f32 s[68:69], s50, v3\n"
    "s_cmp_eq_u64 s[68:69], 0\n"
    "s_cselect_b32 s50, 0, 2\n"
    "s_max_u32 s31, s31, s50\n"
    "s_mov_b32 s50, 0x05040100\n"
    "s_mov_b32 s51, 0x07060302\n"
    "s_waitcnt vmcnt(14)\n"
    "v_perm_b32 v144, v136, v124, s50\n"
    "s_waitcnt vmcnt(12)\n"
    "v_perm_b32 v145, v20, v140, s50\n"
    "s_waitcnt vmcnt(10)\n"
    "v_perm_b32 v146, v80, v28, s50\n"
    "s_waitcnt vmcnt(8)\n"
    "v_perm_b32 v147, v44, v88, s50\n"
    "ds_write_b128 v120, v[144:147] offset:0\n"
    "v_perm_b32 v152, v136, v124, s51\n"
    "v_perm_b32 v153, v20, v140, s51\n"
    "v_perm_b32 v154, v80, v28, s51\n"
    "v_perm_b32 v155, v44, v88, s51\n"
    "ds_write_b128 v27, v[152:155] offset:0\n"
    "v_perm_b32 v4, v137, v125, s50\n"
    "v_perm_b32 v5, v21, v141, s50\n"
    "v_perm_b32 v6, v81, v29, s50\n"
    "v_perm_b32 v7, v45, v89, s50\n"
    "ds_write_b128 v115, v[4:7] offset:0\n"
    "v_perm_b32 v40, v137, v125, s51\n"
    "v_perm_b32 v41, v21, v141, s51\n"
    "v_perm_b32 v42, v81, v29, s51\n"
    "v_perm_b32 v43, v45, v89, s51\n"
    "ds_write_b128 v64, v[40:43] offset:0\n"
    "v_perm_b32 v48, v138, v126, s50\n"
    "v_perm_b32 v49, v22, v142, s50\n"
    "v_perm_b32 v50, v82, v30, s50\n"
    "v_perm_b32 v51, v46, v90, s50\n"
    "ds_write_b128 v2, v[48:51] offset:0\n"
    "v_perm_b32 v132, v138, v126, s51\n"
    "v_perm_b32 v133, v22, v142, s51\n"
    "v_perm_b32 v134, v82, v30, s51\n"
    "v_perm_b32 v135, v46, v90, s51\n"
    "ds_write_b128 v14, v[132:135] offset:0\n"
    "v_perm_b32 v60, v139, v127, s50\n"
    "v_perm_b32 v61, v23, v143, s50\n"
    "v_perm_b32 v62, v83, v31, s50\n"
    "v_perm_b32 v63, v47, v91, s50\n"
    "ds_write_b128 v57, v[60:63] offset:0\n"
    "v_perm_b32 v68, v139, v127, s51\n"
    "v_perm_b32 v69, v23, v143, s51\n"
    "v_perm_b32 v70, v83, v31, s51\n"
    "v_perm_b32 v71, v47, v91, s51\n"
    "ds_write_b128 v121, v[68:71] offset:0\n"
    "v_cmp_gt_u32 s[60:61], 27, v19\n"
    "s_mov_b64 exec, s[60:61]\n"
    "s_waitcnt vmcnt(6)\n"
    "v_perm_b32 v8, v32, v148, s50\n"
    "s_waitcnt vmcnt(4)\n"
    "v_perm_b32 v9, v92, v164, s50\n"
    "s_waitcnt vmcnt(2)\n"
    "v_perm_b32 v10, v160, v84, s50\n"
    "s_waitcnt vmcnt(0)\n"
    "v_perm_b32 v11, v108, v104, s50\n"
    "ds_write_b128 v117, v[8:11] offset:0\n"
    "v_perm_b32 v140, v32, v148, s51\n"
    "v_perm_b32 v141, v92, v164, s51\n"
    "v_perm_b32 v142, v160, v84, s51\n"
    "v_perm_b32 v143, v108, v104, s51\n"
    "ds_write_b128 v112, v[140:143] offset:0\n"
    "v_perm_b32 v140, v33, v149, s50\n"
    "v_perm_b32 v141, v93, v165, s50\n"
    "v_perm_b32 v142, v161, v85, s50\n"
    "v_perm_b32 v143, v109, v105, s50\n"
    "ds_write_b128 v26, v[140:143] offset:0\n"
    "v_perm_b32 v40, v33, v149, s51\n"
    "v_perm_b32 v41, v93, v165, s51\n"
    "v_perm_b32 v42, v161, v85, s51\n"
    "v_perm_b32 v43, v109, v105, s51\n"
    "ds_write_b128 v101, v[40:43] offset:0\n"
    "v_perm_b32 v152, v34, v150, s50\n"
    "v_perm_b32 v153, v94, v166, s50\n"
    "v_perm_b32 v154, v162, v86, s50\n"
    "v_perm_b32 v155, v110, v106, s50\n"
    "ds_write_b128 v159, v[152:155] offset:0\n"
    "v_perm_b32 v40, v34, v150, s51\n"
    "v_perm_b32 v41, v94, v166, s51\n"
    "v_perm_b32 v42, v162, v86, s51\n"
    "v_perm_b32 v43, v110, v106, s51\n"
    "ds_write_b128 v74, v[40:43] offset:0\n"
    "v_perm_b32 v44, v35, v151, s50\n"
    "v_perm_b32 v45, v95, v167, s50\n"
    "v_perm_b32 v46, v163, v87, s50\n"
    "v_perm_b32 v47, v111, v107, s50\n"
    "ds_write_b128 v17, v[44:47] offset:0\n"
    "v_perm_b32 v44, v35, v151, s51\n"
    "v_perm_b32 v45, v95, v167, s51\n"
    "v_perm_b32 v46, v163, v87, s51\n"
    "v_perm_b32 v47, v111, v107, s51\n"
    "ds_write_b128 v118, v[44:47] offset:0\n"
    "s_mov_b64 exec, -1\n"
    "v_mov_b32 v114, 0x27c40\n"
    "v_mov_b32 v51, 1\n"
    "s_mov_b64 exec, 1\n"
    "ds_add_u32 v114, v51 offset:16\n"
    "s_mov_b64 exec, -1\n"
    "s_setprio 0\n"
    "buffer_load_dwordx4 v[0:3], v13, s[24:27], 0 offen offset:0 nt\n"
    "buffer_load_dwordx4 v[4:7], v13, s[24:27], 0 offen offset:64 nt\n"
    "buffer_load_dwordx4 v[8:11], v13, s[24:27], 0 offen offset:128 nt\n"
    "buffer_load_dwordx4 v[12:15], v73, s[24:27], 0 offen offset:0 nt\n"
    "buffer_load_dwordx4 v[16:19], v73, s[24:27], 0 offen offset:64 nt\n"
    "buffer_load_dwordx4 v[20:23], v73, s[24:27], 0 offen offset:128 nt\n"
    "buffer_load_dwordx4 v[24:27], v128, s[24:27], 0 offen offset:0 nt\n"
    "buffer_load_dwordx4 v[28:31], v128, s[24:27], 0 offen offset:64 nt\n"
    "buffer_load_dwordx4 v[32:35], v128, s[24:27], 0 offen offset:128 nt\n"
    "s_setprio 3\n"
    "s_nop 0\n"
    "s_branch .Lpe_%=\n"
    ".Lrc_%=:\n"
    "v_and_b32 v46, 63, v40\n"
    "v_and_b32 v16, 15, v40\n"
    "v_bfe_u32 v71, v40, 4, 2\n"
    "v_and_b32 v111, 48, v40\n"
    "s_sub_i32 s62, %[v], 8\n"
    "s_lshl_b32 s50, %[blk], 2\n"
    "s_mul_i32 s51, %[blk], 12\n"
    "s_sub_i32 s51, s51, 212\n"
    "s_cmp_lt_u32 %[blk], 27\n"
    "s_cselect_b32 s51, s50, s51\n"
    "s_cselect_b32 s60, 0x000f000f, 0\n"
    "s_cselect_b32 s61, 0x000f000f, 0\n"
    "s_cselect_b32 s63, 8, 12\n"
    "s_sub_i32 s51, s51, 4\n"
    "s_cmp_eq_u32 s62, 3\n"
    "s_cselect_b32 s63, 0, s63\n"
    "v_add_u32 v5, s51, v16\n"
    "v_add_u32 v30, s50, v16\n"
    "v_mul_u32_u24 v96, 0x1353, v5\n"
    "v_lshrrev_b32 v96, 18, v96\n"
    "v_mul_u32_u24 v108, 53, v96\n"
    "v_sub_u32 v108, v5, v108\n"
    "v_subrev_u32 v38, 54, v30\n"
    "v_min_u32 v38, 53, v38\n"
    "v_min_u32 v62, 53, v30\n"
    "v_cndmask_b32 v38, v96, v38, s[60:61]\n"
    "v_cndmask_b32 v62, v108, v62, s[60:61]\n"
    "v_add_u32 v30, 0xa8e, v30\n"
    "v_cndmask_b32 v5, v5, v30, s[60:61]\n"
    "v_mad_u32_u24 v38, v38, 7, 9\n"
    "v_subrev_u32 v96, 0x17a, v38\n"
    "v_min_u32 v38, v38, v96\n"
    "v_mad_u32_u24 v62, v62, 7, 9\n"
    "v_subrev_u32 v96, 0x17a, v62\n"
    "v_min_u32 v62, v62, v96\n"
    "s_movk_i32 s50, 378\n"
    "s_movk_i32 s51, 192\n"
    "v_mad_u32_u24 v38, v38, s50, v62\n"
    "v_mad_u32_u24 v122, v38, s51, v111\n"
    "s_movk_i32 s50, 2809\n"
    "v_cmp_gt_u32 s[60:61], s63, v16\n"
    "v_cmp_gt_u32 s[48:49], s50, v5\n"
    "s_and_b64 s[60:61], s[60:61], s[48:49]\n"
    "v_mov_b32 v96, 0x7fffff00\n"
    "v_cndmask_b32 v122, v96, v122, s[60:61]\n"
    "s_cmp_eq_u32 s62, 0\n"
    "s_cselect_b32 s50, 0, 196\n"
    "s_mul_i32 s63, s62, 3136\n"
    "s_sub_i32 s63, s63, 3136\n"
    "s_cmp_eq_u32 s62, 0\n"
    "s_cselect_b32 s63, 9408, s63\n"
    "v_mul_u32_u24 v146, s50, v16\n"
    "v_add_u32 v106, v146, v111\n"
    "s_waitcnt lgkmcnt(0)\n"
    "s_load_dwordx2 s[70:71], s[70:71], 0x0\n"
    "s_mov_b32 s52, s64\n"
    "s_and_b32 s53, s65, 0xffff\n"
    "s_mov_b32 s54, 27433728\n"
    "s_mov_b32 s55, 0x20000\n"
    "s_mov_b32 s76, s78\n"
    "s_and_b32 s77, s79, 0xffff\n"
    "s_mov_b32 s78, 9604\n"
    "s_mov_b32 s79, 0x20000\n"
    "s_mov_b64 s[96:97], s[64:65]\n"
    "s_mov_b64 s[98:99], s[68:69]\n"
    "s_lshl_b32 s50, %[blk], 2\n"
    "s_add_i32 s50, s50, %[v]\n"
    "s_mul_i32 s51, %[blk], 12\n"
    "s_add_i32 s51, s51, %[v]\n"
    "s_sub_i32 s51, s51, 212\n"
    "s_cmp_lt_u32 %[blk], 27\n"
    "s_cselect_b32 s51, s50, s51\n"
    "s_cselect_b32 s60, 8, 12\n"
    "s_cselect_b32 s61, 4, 0\n"
    "s_sub_i32 s51, s51, 4\n"
    "s_mul_i32 s29, s51, 4947\n"
    "s_lshr_b32 s29, s29, 18\n"
    "s_mul_i32 s30, s29, 53\n"
    "s_sub_i32 s30, s51, s30\n"
    "s_sub_i32 s38, s50, 54\n"
    "s_min_u32 s38, s38, 53\n"
    "s_min_u32 s39, s50, 53\n"
    "s_add_i32 s50, s50, 2702\n"
    "s_lshr_b32 s31, %[v], 3\n"
    "s_cmp_lt_u32 %[v], s61\n"
    "s_cselect_b32 s29, s38, s29\n"
    "s_cselect_b32 s30, s39, s30\n"
    "s_cselect_b32 s51, s50, s51\n"
    "s_cselect_b32 s31, 2, s31\n"
    "s_cmp_lt_u32 %[v], s60\n"
    "s_cselect_b32 s51, s51, 9999\n"
    "s_cmp_lt_u32 s51, 2809\n"
    "s_cselect_b32 s31, s31, 3\n"
    "s_mul_i32 s28, s29, 2646\n"
    "s_mul_i32 s50, s30, 7\n"
    "s_add_i32 s28, s28, s50\n"
    "s_add_i32 s28, s28, 1137\n"
    "s_mul_i32 s28, s28, 192\n"
    "s_add_u32 s24, s64, s28\n"
    "s_addc_u32 s25, s65, 0\n"
    "s_and_b32 s25, s25, 0xffff\n"
    "s_sub_i32 s18, 27433728, s28\n"
    "s_cmp_lt_u32 s31, 2\n"
    "s_cselect_b32 s26, s18, 0\n"
    "s_mov_b32 s27, 0x20000\n"
    "s_add_u32 s16, s94, s28\n"
    "s_addc_u32 s17, s95, 0\n"
    "s_and_b32 s17, s17, 0xffff\n"
    "s_mov_b32 s19, 0x20000\n"
    "s_mul_i32 s50, s29, 54\n"
    "s_add_i32 s50, s50, s30\n"
    "s_mul_i32 s50, s50, 4802\n"
    "s_add_u32 s38, s92, s50\n"
    "s_addc_u32 s39, s93, 0\n"
    "s_sub_u32 s20, s38, 57344\n"
    "s_subb_u32 s21, s39, 0\n"
    "s_and_b32 s21, s21, 0xffff\n"
    "s_mov_b32 s22, 62146\n"
    "s_mov_b32 s23, 0x20000\n"
    "s_cmp_eq_u32 s29, 53\n"
    "s_cselect_b32 s29, 4, 99\n"
    "s_cmp_eq_u32 s30, 53\n"
    "s_cselect_b32 s30, 4, 99\n"
    "s_and_b32 s34, %[v], 3\n"
    "s_mul_i32 s34, s34, 4816\n"
    "s_add_i32 s34, s34, 143616\n"
    "s_movk_i32 s37, 4096\n"
    "s_mov_b32 s40, 0x2a0cbccc\n"
    "s_mov_b32 s41, 0x2a0cbccc\n"
    "s_mov_b32 s42, 0x387ba882\n"
    "s_mov_b32 s43, 0x387ba882\n"
    "s_setprio 2\n"
    "s_barrier\n"
    "buffer_load_dwordx4 v[88:91], v106, s[76:79], s63 offen offset:0\n"
    "buffer_load_dwordx4 v[164:167], v106, s[76:79], s63 offen offset:64\n"
    "buffer_load_dwordx4 v[128:131], v106, s[76:79], s63 offen offset:128\n"
    "buffer_load_dword v145, v146, s[76:79], s63 offen offset:192\n"
    "buffer_load_dwordx4 v[152:155], v122, s[52:55], 0 offen offset:0 nt\n"
    "buffer_load_dwordx4 v[156:159], v122, s[52:55], 0 offen offset:64 nt\n"
    "buffer_load_dwordx4 v[24:27], v122, s[52:55], 0 offen offset:128 nt\n"
    "s_movk_i32 s50, 371\n"
    "s_movk_i32 s51, 192\n"
    "v_add_u32 v12, 0, v16\n"
    "v_mul_u32_u24 v70, 37, v12\n"
    "v_lshrrev_b32 v70, 8, v70\n"
    "v_mad_u32_u24 v70, v70, s50, v12\n"
    "v_mad_u32_u24 v94, v70, s51, v111\n"
    "v_add_u32 v139, 16, v16\n"
    "v_mul_u32_u24 v137, 37, v139\n"
    "v_lshrrev_b32 v137, 8, v137\n"
    "v_mad_u32_u24 v137, v137, s50, v139\n"
    "v_mad_u32_u24 v117, v137, s51, v111\n"
    "v_add_u32 v137, 32, v16\n"
    "v_mul_u32_u24 v2, 37, v137\n"
    "v_lshrrev_b32 v2, 8, v2\n"
    "v_mad_u32_u24 v2, v2, s50, v137\n"
    "v_mad_u32_u24 v106, v2, s51, v111\n"
    "v_lshrrev_b32 v44, 1, v16\n"
    "v_and_b32 v44, 4, v44\n"
    "v_xor_b32 v44, v44, v16\n"
    "v_xor_b32 v44, v44, v71\n"
    "v_add_u32 v44, v44, v111\n"
    "v_lshlrev_b32 v36, 4, v44\n"
    "v_lshlrev_b32 v37, 4, v46\n"
    "v_add_u32 v37, 0xe000, v37\n"
    "s_mul_i32 s50, %[v], 1472\n"
    "s_add_i32 s50, s50, 125952\n"
    "v_add_u32 v38, s50, v111\n"
    "v_and_b32 v79, 1, v16\n"
    "v_lshlrev_b32 v79, 4, v79\n"
    "v_mov_b32 v163, 0x3f80\n"
    "v_lshlrev_b32 v79, v79, v163\n"
    "s_mov_b32 s60, 0xcccccccc\n"
    "s_mov_b32 s61, 0xcccccccc\n"
    "v_cndmask_b32 v22, v79, 0, s[60:61]\n"
    "v_cndmask_b32 v23, 0, v79, s[60:61]\n"
    "s_movk_i32 s51, 192\n"
    "v_bfe_u32 v33, v16, 1, 1\n"
    "v_mul_u32_u24 v33, 24, v33\n"
    "v_lshrrev_b32 v109, 2, v16\n"
    "v_lshl_add_u32 v33, v109, 1, v33\n"
    "v_and_b32 v109, 1, v16\n"
    "v_add3_u32 v33, v33, v109, 16\n"
    "v_mad_u32_u24 v33, v33, s51, v111\n"
    "v_mad_u32_u24 v109, v16, s51, v111\n"
    "s_cmp_lt_u32 s62, 2\n"
    "s_cselect_b32 s74, 1, 0\n"
    "s_xor_b32 s74, s74, s62\n"
    "s_cmp_eq_u32 s74, 3\n"
    "s_cselect_b64 s[60:61], -1, 0\n"
    "s_cmp_eq_u32 s74, 1\n"
    "s_cselect_b64 s[64:65], -1, 0\n"
    "s_sub_i32 s50, s74, 1\n"
    "s_mul_i32 s72, s50, 4608\n"
    "s_add_i32 s73, s72, 4608\n"
    "s_cmp_eq_u32 s74, 1\n"
    "s_cselect_b32 s73, 0, s73\n"
    "s_max_i32 s72, s72, 0\n"
    "s_lshl_b32 s50, s50, 10\n"
    "v_cndmask_b32 v92, v109, v33, s[60:61]\n"
    "v_cndmask_b32 v75, v109, v33, s[64:65]\n"
    "v_add_u32 v74, s50, v37\n"
    "s_waitcnt lgkmcnt(0)\n"
    "s_mov_b32 s56, s98\n"
    "s_and_b32 s57, s99, 0xffff\n"
    "s_cmp_eq_u32 s74, 0\n"
    "s_cselect_b32 s58, 0, 18432\n"
    "s_mov_b32 s59, 0x20000\n"
    "v_mov_b32 v80, 0x3f803f80\n"
    "v_mov_b32 v81, 0x3f803f80\n"
    "v_mov_b32 v82, 0x3f803f80\n"
    "v_mov_b32 v83, 0x3f803f80\n"
    "v_mov_b32 v100, 0\n"
    "v_mov_b32 v101, 0\n"
    "v_mov_b32 v104, 0\n"
    "v_mov_b32 v105, 0\n"
    "s_mov_b32 s60, 0x00f0000f\n"
    "s_mov_b32 s61, 0xf0000f00\n"
    "v_cndmask_b32 v102, 0, v22, s[60:61]\n"
    "v_cndmask_b32 v103, 0, v23, s[60:61]\n"
    "s_mov_b32 s68, 0\n"
    "s_mov_b32 s69, -1\n"
    "s_waitcnt lgkmcnt(0)\n"
    "v_mov_b32 v45, s70\n"
    "v_mov_b32 v125, s71\n"
    "v_cndmask_b32 v45, v45, v125, s[68:69]\n"
    "s_mov_b32 s68, 0xffff0000\n"
    "s_mov_b32 s69, 0xffff0000\n"
    "v_lshlrev_b32 v125, 16, v45\n"
    "v_and_b32 v45, 0xffff0000, v45\n"
    "v_cndmask_b32 v45, v125, v45, s[68:69]\n"
    "v_max_f32 v45, 0xc2c80000, v45\n"
    "v_min_f32 v45, 0x40935dcc, v45\n"
    "v_cvt_pk_bf16_f32 v45, v45, 0\n"
    "v_lshlrev_b32 v45, 16, v45\n"
    "v_mul_f32 v45, 0x3fb8aa3b, v45\n"
    "v_exp_f32 v45, v45\n"
    "s_nop 0\n"
    "v_cvt_pk_bf16_f32 v45, v45, 0\n"
    "v_lshlrev_b32 v45, 16, v45\n"
    "v_mul_f32 v10, 0x3fb8aa3b, v45\n"
    "s_mov_b32 s50, 0x41e6d4ca\n"
    "s_nop 0\n"
    "v_readlane_b32 s44, v10, 0\n"
    "v_readlane_b32 s45, v10, 16\n"
    "v_readlane_b32 s46, v10, 32\n"
    "v_readlane_b32 s47, v10, 48\n"
    "v_cmp_nge_f32 s[68:69], s50, v10\n"
    "s_cmp_eq_u64 s[68:69], 0\n"
    "s_cselect_b32 s50, 0, 2\n"
    "s_max_u32 s31, s31, s50\n"
    "v_mov_b32 v53, 0xff800000\n"
    "s_sub_i32 s50, s62, 1\n"
    "s_lshl_b32 s51, s50, 2\n"
    "s_mul_i32 s50, s50, 3072\n"
    "s_mov_b32 s64, 0xffff0000\n"
    "s_mov_b32 s65, -1\n"
    "v_add_u32 v18, s50, v37\n"
    "v_add_u32 v58, s51, v37\n"
    "s_mov_b32 s50, 122880\n"
    "s_mov_b32 s60, 0xaaaaaaaa\n"
    "s_mov_b32 s61, 0xaaaaaaaa\n"
    "s_waitcnt vmcnt(6)\n"
    "v_lshl_add_u32 v88, v88, 4, s50\n"
    "v_lshl_add_u32 v89, v89, 4, s50\n"
    "v_lshl_add_u32 v90, v90, 4, s50\n"
    "v_lshl_add_u32 v91, v91, 4, s50\n"
    "s_waitcnt vmcnt(5)\n"
    "v_lshl_add_u32 v164, v164, 4, s50\n"
    "v_lshl_add_u32 v165, v165, 4, s50\n"
    "v_lshl_add_u32 v166, v166, 4, s50\n"
    "v_lshl_add_u32 v167, v167, 4, s50\n"
    "s_waitcnt vmcnt(4)\n"
    "v_lshl_add_u32 v128, v128, 4, s50\n"
    "v_lshl_add_u32 v129, v129, 4, s50\n"
    "v_lshl_add_u32 v130, v130, 4, s50\n"
    "v_lshl_add_u32 v131, v131, 4, s50\n"
    "s_waitcnt vmcnt(3)\n"
    "v_lshl_add_u32 v145, v145, 4, s50\n"
    "ds_read_b128 v[112:115], v88 offset:0\n"
    "ds_read_b128 v[60:63], v89 offset:0\n"
    "ds_read_b128 v[48:51], v90 offset:0\n"
    "ds_read_b128 v[12:15], v91 offset:0\n"
    "ds_read_b128 v[64:67], v164 offset:0\n"
    "ds_read_b128 v[0:3], v165 offset:0\n"
    "ds_read_b128 v[32:35], v166 offset:0\n"
    "ds_read_b128 v[96:99], v167 offset:0\n"
    "ds_read_b128 v[28:31], v128 offset:0\n"
    "ds_read_b128 v[132:135], v129 offset:0\n"
    "ds_read_b128 v[8:11], v130 offset:0\n"
    "ds_read_b128 v[120:123], v131 offset:0\n"
    "ds_read_b128 v[164:167], v145 offset:0\n"
    "s_waitcnt lgkmcnt(0)\n"
    "v_cndmask_b32 v164, v164, v53, s[64:65]\n"
    "v_cndmask_b32 v165, v165, v53, s[64:65]\n"
    "v_cndmask_b32 v166, v166, v53, s[64:65]\n"
    "v_cndmask_b32 v167, v167, v53, s[64:65]\n"
    "s_waitcnt lgkmcnt(0)\n"
    "s_cmp_eq_u32 s62, 0\n"
    "s_cbranch_scc0 .Lbm_%=\n"
    "v_cndmask_b32 v76, v112, v113, s[60:61]\n"
    "v_cndmask_b32 v77, v60, v61, s[60:61]\n"
    "v_cndmask_b32 v78, v48, v49, s[60:61]\n"
    "v_cndmask_b32 v79, v12, v13, s[60:61]\n"
    "v_cndmask_b32 v144, v64, v65, s[60:61]\n"
    "v_cndmask_b32 v145, v0, v1, s[60:61]\n"
    "v_cndmask_b32 v146, v32, v33, s[60:61]\n"
    "v_cndmask_b32 v147, v96, v97, s[60:61]\n"
    "v_cndmask_b32 v160, v28, v29, s[60:61]\n"
    "v_cndmask_b32 v161, v132, v133, s[60:61]\n"
    "v_cndmask_b32 v162, v8, v9, s[60:61]\n"
    "v_cndmask_b32 v163, v120, v121, s[60:61]\n"
    "v_cndmask_b32 v40, v164, v165, s[60:61]\n"
    "v_cndmask_b32 v136, v114, v115, s[60:61]\n"
    "v_cndmask_b32 v137, v62, v63, s[60:61]\n"
    "v_cndmask_b32 v138, v50, v51, s[60:61]\n"
    "v_cndmask_b32 v139, v14, v15, s[60:61]\n"
    "v_cndmask_b32 v124, v66, v67, s[60:61]\n"
    "v_cndmask_b32 v125, v2, v3, s[60:61]\n"
    "v_cndmask_b32 v126, v34, v35, s[60:61]\n"
    "v_cndmask_b32 v127, v98, v99, s[60:61]\n"
    "v_cndmask_b32 v148, v30, v31, s[60:61]\n"
    "v_cndmask_b32 v149, v134, v135, s[60:61]\n"
    "v_cndmask_b32 v150, v10, v11, s[60:61]\n"
    "v_cndmask_b32 v151, v122, v123, s[60:61]\n"
    "v_cndmask_b32 v47, v166, v167, s[60:61]\n"
    "s_mov_b32 exec_lo, 0xfffcfffc\n"
    "s_mov_b32 exec_hi, 0xfffcfffc\n"
    "v_mov_b32 v76, v53\n"
    "v_mov_b32 v77, v53\n"
    "v_mov_b32 v78, v53\n"
    "v_mov_b32 v79, v53\n"
    "v_mov_b32 v144, v53\n"
    "v_mov_b32 v145, v53\n"
    "v_mov_b32 v146, v53\n"
    "v_mov_b32 v147, v53\n"
    "v_mov_b32 v160, v53\n"
    "v_mov_b32 v161, v53\n"
    "v_mov_b32 v162, v53\n"
    "v_mov_b32 v163, v53\n"
    "v_mov_b32 v136, v53\n"
    "v_mov_b32 v137, v53\n"
    "v_mov_b32 v138, v53\n"
    "v_mov_b32 v139, v53\n"
    "v_mov_b32 v124, v53\n"
    "v_mov_b32 v125, v53\n"
    "v_mov_b32 v126, v53\n"
    "v_mov_b32 v127, v53\n"
    "v_mov_b32 v148, v53\n"
    "v_mov_b32 v149, v53\n"
    "v_mov_b32 v150, v53\n"
    "v_mov_b32 v151, v53\n"
    "v_mov_b32 v40, v53\n"
    "v_mov_b32 v47, v53\n"
    "s_mov_b64 exec, -1\n"
    "ds_write_b128 v37, v[76:79] offset:59392\n"
    "ds_write_b128 v37, v[144:147] offset:60416\n"
    "ds_write_b128 v37, v[160:163] offset:61440\n"
    "ds_write_b32 v37, v40 offset:55308\n"
    "ds_write_b128 v37, v[136:139] offset:62464\n"
    "ds_write_b128 v37, v[124:127] offset:63488\n"
    "ds_write_b128 v37, v[148:151] offset:64512\n"
    "ds_write_b32 v37, v47 offset:57356\n"
    "s_waitcnt lgkmcnt(0)\n"
    "s_branch .Lbe_%=\n"
    ".Lbm_%=:\n"
    "v_swap_b32 v113, v60\n"
    "v_swap_b32 v114, v48\n"
    "v_swap_b32 v115, v12\n"
    "v_swap_b32 v62, v49\n"
    "v_swap_b32 v63, v13\n"
    "v_swap_b32 v51, v14\n"
    "v_swap_b32 v65, v0\n"
    "v_swap_b32 v66, v32\n"
    "v_swap_b32 v67, v96\n"
    "v_swap_b32 v2, v33\n"
    "v_swap_b32 v3, v97\n"
    "v_swap_b32 v35, v98\n"
    "v_swap_b32 v29, v132\n"
    "v_swap_b32 v30, v8\n"
    "v_swap_b32 v31, v120\n"
    "v_swap_b32 v134, v9\n"
    "v_swap_b32 v135, v121\n"
    "v_swap_b32 v11, v122\n"
    "ds_write_b128 v18, v[112:115] offset:18432\n"
    "ds_write_b128 v18, v[64:67] offset:19456\n"
    "ds_write_b128 v18, v[28:31] offset:20480\n"
    "ds_write_b32 v58, v164 offset:55296\n"
    "ds_write_b128 v18, v[60:63] offset:27648\n"
    "ds_write_b128 v18, v[0:3] offset:28672\n"
    "ds_write_b128 v18, v[132:135] offset:29696\n"
    "ds_write_b32 v58, v165 offset:56320\n"
    "ds_write_b128 v18, v[48:51] offset:36864\n"
    "ds_write_b128 v18, v[32:35] offset:37888\n"
    "ds_write_b128 v18, v[8:11] offset:38912\n"
    "ds_write_b32 v58, v166 offset:57344\n"
    "ds_write_b128 v18, v[12:15] offset:46080\n"
    "ds_write_b128 v18, v[96:99] offset:47104\n"
    "ds_write_b128 v18, v[120:123] offset:48128\n"
    "ds_write_b32 v58, v167 offset:58368\n"
    "s_waitcnt lgkmcnt(0)\n"
    ".Lbe_%=:\n"
    "s_min_u32 s50, s62, 2\n"
    "s_mul_i32 s50, s50, 18432\n"
    "s_movk_i32 s51, 1472\n"
    "v_add_u32 v98, s50, v36\n"
    "v_mad_u32_u24 v73, v16, s51, v111\n"
    "v_add_u32 v73, 0x1ec00, v73\n"
    "s_waitcnt vmcnt(0)\n"
    "s_cmp_eq_u32 s62, 3\n"
    "s_cbranch_scc1 .Loe_%=\n"
    "v_mov_b32 v144, 0x27c40\n"
    ".Lp3_%=:\n"
    "ds_read_b32 v0, v144 offset:16\n"
    "s_waitcnt lgkmcnt(0)\n"
    "v_readfirstlane_b32 s50, v0\n"
    "s_cmp_ge_i32 s50, 5\n"
    "s_cbranch_scc1 .Lq3_%=\n"
    "s_sleep 1\n"
    "s_branch .Lp3_%=\n"
    ".Lq3_%=:\n"
    "s_setprio 2\n"
    "ds_read_b128 v[8:11], v98 offset:0\n"
    "ds_read_b128 v[16:19], v98 offset:3072\n"
    "ds_read_b128 v[52:55], v98 offset:6144\n"
    "ds_read_b128 v[56:59], v98 offset:9216\n"
    "ds_read_b128 v[4:7], v98 offset:12288\n"
    "ds_read_b128 v[136:139], v98 offset:15360\n"
    "ds_read_b128 v[108:111], v98 offset:1024\n"
    "ds_read_b128 v[140:143], v98 offset:4096\n"
    "ds_read_b128 v[148:151], v98 offset:7168\n"
    "ds_read_b128 v[164:167], v98 offset:10240\n"
    "ds_read_b128 v[132:135], v98 offset:13312\n"
    "ds_read_b128 v[48:51], v98 offset:16384\n"
    "ds_read_b128 v[0:3], v98 offset:2048\n"
    "ds_read_b128 v[84:87], v98 offset:5120\n"
    "ds_read_b128 v[112:115], v98 offset:8192\n"
    "ds_read_b128 v[88:91], v98 offset:11264\n"
    "ds_read_b128 v[144:147], v98 offset:14336\n"
    "ds_read_b128 v[120:123], v98 offset:17408\n"
    "s_waitcnt lgkmcnt(14)\n"
    "v_mfma_f32_16x16x32_bf16 v[12:15], v[8:11], v[152:155], 0\n"
    "v_mfma_f32_16x16x32_bf16 v[68:71], v[16:19], v[152:155], 0\n"
    "v_mfma_f32_16x16x32_bf16 v[40:43], v[52:55], v[152:155], 0\n"
    "v_mfma_f32_16x16x32_bf16 v[52:55], v[56:59], v[152:155], 0\n"
    "s_waitcnt lgkmcnt(13)\n"
    "v_mfma_f32_16x16x32_bf16 v[44:47], v[4:7], v[152:155], 0\n"
    "s_waitcnt lgkmcnt(12)\n"
    "v_mfma_f32_16x16x32_bf16 v[56:59], v[136:139], v[152:155], 0\n"
    "s_waitcnt lgkmcnt(11)\n"
    "v_mfma_f32_16x16x32_bf16 v[12:15], v[108:111], v[156:159], v[12:15]\n"
    "s_waitcnt lgkmcnt(10)\n"
    "v_mfma_f32_16x16x32_bf16 v[68:71], v[140:143], v[156:159], v[68:71]\n"
    "s_waitcnt lgkmcnt(9)\n"
    "v_mfma_f32_16x16x32_bf16 v[40:43], v[148:151], v[156:159], v[40:43]\n"
    "s_waitcnt lgkmcnt(8)\n"
    "v_mfma_f32_16x16x32_bf16 v[52:55], v[164:167], v[156:159], v[52:55]\n"
    "s_waitcnt lgkmcnt(7)\n"
    "v_mfma_f32_16x16x32_bf16 v[44:47], v[132:135], v[156:159], v[44:47]\n"
    "s_waitcnt lgkmcnt(6)\n"
    "v_mfma_f32_16x16x32_bf16 v[56:59], v[48:51], v[156:159], v[56:59]\n"
    "s_waitcnt lgkmcnt(5)\n"
    "v_mfma_f32_16x16x32_bf16 v[12:15], v[0:3], v[24:27], v[12:15]\n"
    "s_waitcnt lgkmcnt(4)\n"
    "v_mfma_f32_16x16x32_bf16 v[68:71], v[84:87], v[24:27], v[68:71]\n"
    "s_waitcnt lgkmcnt(3)\n"
    "v_mfma_f32_16x16x32_bf16 v[40:43], v[112:115], v[24:27], v[40:43]\n"
    "s_waitcnt lgkmcnt(2)\n"
    "v_mfma_f32_16x16x32_bf16 v[52:55], v[88:91], v[24:27], v[52:55]\n"
    "s_waitcnt lgkmcnt(1)\n"
    "v_mfma_f32_16x16x32_bf16 v[44:47], v[144:147], v[24:27], v[44:47]\n"
    "s_waitcnt lgkmcnt(0)\n"
    "v_mfma_f32_16x16x32_bf16 v[56:59], v[120:123], v[24:27], v[56:59]\n"
    "s_waitcnt lgkmcnt(0)\n"
    "s_nop 7\n"
    "s_mov_b32 s60, 0x0fff0fff\n"
    "s_mov_b32 s61, 0x0fff0fff\n"
    "s_cmp_lt_u32 s62, 2\n"
    "s_cbranch_scc0 .Lov_%=\n"
    "v_fmaak_f32 v24, v12, v12, 0x2a8cbccc\n"
    "v_fmaak_f32 v152, v40, v40, 0x2a8cbccc\n"
    "s_nop 0\n"
    "v_fmaak_f32 v108, v52, v52, 0x2a8cbccc\n"
    "s_nop 2\n"
    "v_fmaak_f32 v86, v56, v56, 0x2a8cbccc\n"
    "v_fmac_f32 v24, v13, v13\n"
    "v_fmac_f32 v152, v41, v41\n"
    "v_fmac_f32 v108, v53, v53\n"
    "v_fmac_f32 v86, v57, v57\n"
    "v_fmac_f32 v24, v14, v14\n"
    "v_fmac_f32 v152, v42, v42\n"
    "v_fmac_f32 v108, v54, v54\n"
    "v_fmac_f32 v86, v58, v58\n"
    "v_fmac_f32 v24, v15, v15\n"
    "v_fmac_f32 v152, v43, v43\n"
    "v_fmac_f32 v108, v55, v55\n"
    "v_fmac_f32 v86, v59, v59\n"
    "v_fmac_f32 v24, v68, v68\n"
    "v_fmac_f32 v152, v70, v70\n"
    "v_fmac_f32 v108, v44, v44\n"
    "v_fmac_f32 v86, v46, v46\n"
    "v_fmac_f32 v24, v69, v69\n"
    "v_fmac_f32 v152, v71, v71\n"
    "v_fmac_f32 v108, v45, v45\n"
    "v_fmac_f32 v86, v47, v47\n"
    "v_cvt_pk_bf16_f32 v136, v24, v152\n"
    "v_cvt_pk_bf16_f32 v137, v108, v86\n"
    "s_nop 1\n"
    "v_mfma_f32_16x16x16_bf16 v[84:87], v[22:23], v[136:137], 0\n"
    "s_cmp_eq_u32 s62, 0\n"
    "s_cselect_b32 s64, s44, 1.0\n"
    "s_cselect_b32 s65, s45, 1.0\n"
    "s_cselect_b32 s66, s46, 1.0\n"
    "s_cselect_b32 s67, s47, 1.0\n"
    "s_cselect_b32 s50, 256, 0\n"
    "v_add_u32 v98, s50, v73\n"
    "s_nop 5\n"
    "v_rsq_f32 v84, v84\n"
    "v_rsq_f32 v85, v85\n"
    "v_rsq_f32 v86, v86\n"
    "v_rsq_f32 v87, v87\n"
    "v_mul_f32 v84, s64, v84\n"
    "v_mul_f32 v85, s65, v85\n"
    "v_mul_f32 v86, s66, v86\n"
    "v_mul_f32 v87, s67, v87\n"
    "v_mul_f32 v12, v12, v84\n"
    "v_mul_f32 v13, v13, v84\n"
    "v_mul_f32 v14, v14, v84\n"
    "v_mul_f32 v15, v15, v84\n"
    "v_mul_f32 v68, v68, v84\n"
    "v_mul_f32 v69, v69, v84\n"
    "v_cvt_pk_bf16_f32 v144, v12, v13\n"
    "v_cvt_pk_bf16_f32 v145, v14, v15\n"
    "v_cvt_pk_bf16_f32 v146, v68, v69\n"
    "v_mov_b32 v147, 0\n"
    "v_mul_f32 v40, v40, v85\n"
    "v_mul_f32 v41, v41, v85\n"
    "v_mul_f32 v42, v42, v85\n"
    "v_mul_f32 v43, v43, v85\n"
    "v_mul_f32 v70, v70, v85\n"
    "v_mul_f32 v71, v71, v85\n"
    "v_cvt_pk_bf16_f32 v50, v40, v41\n"
    "v_cvt_pk_bf16_f32 v51, v42, v43\n"
    "v_cvt_pk_bf16_f32 v49, v70, v71\n"
    "v_mov_b32 v48, 0\n"
    "v_mul_f32 v52, v52, v86\n"
    "v_mul_f32 v53, v53, v86\n"
    "v_mul_f32 v54, v54, v86\n"
    "v_mul_f32 v55, v55, v86\n"
    "v_mul_f32 v44, v44, v86\n"
    "v_mul_f32 v45, v45, v86\n"
    "v_cvt_pk_bf16_f32 v164, v52, v53\n"
    "v_cvt_pk_bf16_f32 v165, v54, v55\n"
    "v_cvt_pk_bf16_f32 v166, v44, v45\n"
    "v_mov_b32 v167, 0\n"
    "v_mul_f32 v56, v56, v87\n"
    "v_mul_f32 v57, v57, v87\n"
    "v_mul_f32 v58, v58, v87\n"
    "v_mul_f32 v59, v59, v87\n"
    "v_mul_f32 v46, v46, v87\n"
    "v_mul_f32 v47, v47, v87\n"
    "v_cvt_pk_bf16_f32 v162, v56, v57\n"
    "v_cvt_pk_bf16_f32 v163, v58, v59\n"
    "v_cvt_pk_bf16_f32 v161, v46, v47\n"
    "v_mov_b32 v160, 0\n"
    "s_mov_b64 exec, s[60:61]\n"
    "ds_write_b128 v98, v[144:147] offset:0\n"
    "ds_write_b128 v98, v[48:51] offset:64\n"
    "ds_write_b128 v98, v[164:167] offset:128\n"
    "ds_write_b128 v98, v[160:163] offset:192\n"
    "s_mov_b64 exec, -1\n"
    "s_waitcnt lgkmcnt(0)\n"
    "s_nop 7\n"
    "s_branch .Loe_%=\n"
    ".Lov_%=:\n"
    "v_cvt_pk_bf16_f32 v12, v12, 0\n"
    "v_cvt_pk_bf16_f32 v13, v13, 0\n"
    "v_cvt_pk_bf16_f32 v14, v14, 0\n"
    "v_cvt_pk_bf16_f32 v15, v15, 0\n"
    "v_cvt_pk_bf16_f32 v68, v68, 0\n"
    "v_cvt_pk_bf16_f32 v69, v69, 0\n"
    "v_cvt_pk_bf16_f32 v70, v70, 0\n"
    "v_cvt_pk_bf16_f32 v71, v71, 0\n"
    "v_cvt_pk_bf16_f32 v40, v40, 0\n"
    "v_cvt_pk_bf16_f32 v41, v41, 0\n"
    "v_cvt_pk_bf16_f32 v42, v42, 0\n"
    "v_cvt_pk_bf16_f32 v43, v43, 0\n"
    "v_cvt_pk_bf16_f32 v52, v52, 0\n"
    "v_cvt_pk_bf16_f32 v53, v53, 0\n"
    "v_cvt_pk_bf16_f32 v54, v54, 0\n"
    "v_cvt_pk_bf16_f32 v55, v55, 0\n"
    "v_cvt_pk_bf16_f32 v44, v44, 0\n"
    "v_cvt_pk_bf16_f32 v45, v45, 0\n"
    "v_cvt_pk_bf16_f32 v46, v46, 0\n"
    "v_cvt_pk_bf16_f32 v47, v47, 0\n"
    "v_cvt_pk_bf16_f32 v56, v56, 0\n"
    "v_cvt_pk_bf16_f32 v57, v57, 0\n"
    "v_cvt_pk_bf16_f32 v58, v58, 0\n"
    "v_cvt_pk_bf16_f32 v59, v59, 0\n"
    "s_mov_b64 exec, s[60:61]\n"
    "ds_write_b128 v73, v[12:15] offset:1024\n"
    "ds_write_b128 v73, v[68:71] offset:1088\n"
    "ds_write_b128 v73, v[40:43] offset:1152\n"
    "ds_write_b128 v73, v[52:55] offset:1216\n"
    "ds_write_b128 v73, v[44:47] offset:1280\n"
    "ds_write_b128 v73, v[56:59] offset:1344\n"
    "s_mov_b64 exec, -1\n"
    "s_waitcnt lgkmcnt(0)\n"
    "s_nop 7\n"
    ".Loe_%=:\n"
    "v_mov_b32 v65, 0x27c40\n"
    "v_mov_b32 v79, 1\n"
    "s_mov_b64 exec, 1\n"
    "ds_add_u32 v65, v79 offset:0\n"
    "s_mov_b64 exec, -1\n"
    "s_setprio 0\n"
    "buffer_load_dwordx4 v[64:67], v92, s[56:59], s72 offen offset:0\n"
    "buffer_load_dwordx4 v[148:151], v92, s[56:59], s72 offen offset:64\n"
    "buffer_load_dwordx4 v[56:59], v92, s[56:59], s72 offen offset:128\n"
    "buffer_load_dwordx4 v[152:155], v75, s[56:59], s73 offen offset:0\n"
    "buffer_load_dwordx4 v[68:71], v75, s[56:59], s73 offen offset:64\n"
    "buffer_load_dwordx4 v[164:167], v75, s[56:59], s73 offen offset:128\n"
    "buffer_load_dwordx4 v[0:3], v94, s[24:27], 0 offen offset:0 nt\n"
    "buffer_load_dwordx4 v[4:7], v94, s[24:27], 0 offen offset:64 nt\n"
    "buffer_load_dwordx4 v[8:11], v94, s[24:27], 0 offen offset:128 nt\n"
    "buffer_load_dwordx4 v[12:15], v117, s[24:27], 0 offen offset:0 nt\n"
    "buffer_load_dwordx4 v[16:19], v117, s[24:27], 0 offen offset:64 nt\n"
    "buffer_load_dwordx4 v[20:23], v117, s[24:27], 0 offen offset:128 nt\n"
    "buffer_load_dwordx4 v[24:27], v106, s[24:27], 0 offen offset:0 nt\n"
    "buffer_load_dwordx4 v[28:31], v106, s[24:27], 0 offen offset:64 nt\n"
    "buffer_load_dwordx4 v[32:35], v106, s[24:27], 0 offen offset:128 nt\n"
    "s_setprio 1\n"
    "s_waitcnt vmcnt(9)\n"
    "s_cmp_eq_u32 s74, 0\n"
    "s_cbranch_scc1 .Lwe_%=\n"
    "v_mfma_f32_16x16x32_bf16 v[96:99], v[64:67], v[80:83], 0\n"
    "v_mfma_f32_16x16x32_bf16 v[116:119], v[152:155], v[80:83], 0\n"
    "v_mfma_f32_16x16x32_bf16 v[96:99], v[148:151], v[80:83], v[96:99]\n"
    "v_mfma_f32_16x16x32_bf16 v[116:119], v[68:71], v[80:83], v[116:119]\n"
    "v_mfma_f32_16x16x32_bf16 v[96:99], v[56:59], v[80:83], v[96:99]\n"
    "v_mfma_f32_16x16x32_bf16 v[116:119], v[164:167], v[80:83], v[116:119]\n"
    "s_nop 6\n"
    "v_mul_f32 v96, 0xbc2aaaab, v96\n"
    "v_mul_f32 v97, 0xbc2aaaab, v97\n"
    "v_mul_f32 v98, 0xbc2aaaab, v98\n"
    "v_mul_f32 v99, 0xbc2aaaab, v99\n"
    "v_mul_f32 v116, 0xbc2aaaab, v116\n"
    "v_mul_f32 v117, 0xbc2aaaab, v117\n"
    "v_mul_f32 v118, 0xbc2aaaab, v118\n"
    "v_mul_f32 v119, 0xbc2aaaab, v119\n"
    "v_mfma_f32_16x16x32_bf16 v[128:131], v[64:67], v[102:105], v[96:99]\n"
    "s_nop 0\n"
    "v_mfma_f32_16x16x32_bf16 v[92:95], v[152:155], v[102:105], v[116:119]\n"
    "v_mfma_f32_16x16x32_bf16 v[76:79], v[64:67], v[100:103], v[96:99]\n"
    "v_mfma_f32_16x16x32_bf16 v[112:115], v[152:155], v[100:103], v[116:119]\n"
    "v_mfma_f32_16x16x32_bf16 v[136:139], v[148:151], v[102:105], v[96:99]\n"
    "v_mfma_f32_16x16x32_bf16 v[120:123], v[68:71], v[102:105], v[116:119]\n"
    "v_mfma_f32_16x16x32_bf16 v[60:63], v[148:151], v[100:103], v[96:99]\n"
    "v_mfma_f32_16x16x32_bf16 v[140:143], v[68:71], v[100:103], v[116:119]\n"
    "v_mfma_f32_16x16x32_bf16 v[80:83], v[56:59], v[102:105], v[96:99]\n"
    "v_mfma_f32_16x16x32_bf16 v[124:127], v[164:167], v[102:105], v[116:119]\n"
    "v_mfma_f32_16x16x32_bf16 v[148:151], v[56:59], v[100:103], v[96:99]\n"
    "v_mfma_f32_16x16x32_bf16 v[64:67], v[164:167], v[100:103], v[116:119]\n"
    "v_cvt_pk_bf16_f32 v56, v128, v129\n"
    "v_cvt_pk_bf16_f32 v57, v130, v131\n"
    "v_cvt_pk_bf16_f32 v58, v92, v93\n"
    "v_cvt_pk_bf16_f32 v59, v94, v95\n"
    "ds_write_b128 v74, v[56:59] offset:0\n"
    "v_cvt_pk_bf16_f32 v40, v76, v77\n"
    "v_cvt_pk_bf16_f32 v41, v78, v79\n"
    "v_cvt_pk_bf16_f32 v42, v112, v113\n"
    "v_cvt_pk_bf16_f32 v43, v114, v115\n"
    "ds_write_b128 v74, v[40:43] offset:3072\n"
    "v_cvt_pk_bf16_f32 v160, v136, v137\n"
    "v_cvt_pk_bf16_f32 v161, v138, v139\n"
    "v_cvt_pk_bf16_f32 v162, v120, v121\n"
    "v_cvt_pk_bf16_f32 v163, v122, v123\n"
    "ds_write_b128 v74, v[160:163] offset:6144\n"
    "v_cvt_pk_bf16_f32 v44, v60, v61\n"
    "v_cvt_pk_bf16_f32 v45, v62, v63\n"
    "v_cvt_pk_bf16_f32 v46, v140, v141\n"
    "v_cvt_pk_bf16_f32 v47, v142, v143\n"
    "ds_write_b128 v74, v[44:47] offset:9216\n"
    "v_cvt_pk_bf16_f32 v160, v80, v81\n"
    "v_cvt_pk_bf16_f32 v161, v82, v83\n"
    "v_cvt_pk_bf16_f32 v162, v124, v125\n"
    "v_cvt_pk_bf16_f32 v163, v126, v127\n"
    "ds_write_b128 v74, v[160:163] offset:12288\n"
    "v_cvt_pk_bf16_f32 v140, v148, v149\n"
    "v_cvt_pk_bf16_f32 v141, v150, v151\n"
    "v_cvt_pk_bf16_f32 v142, v64, v65\n"
    "v_cvt_pk_bf16_f32 v143, v66, v67\n"
    "ds_write_b128 v74, v[140:143] offset:15360\n"
    "v_mov_b32 v98, 0x27c40\n"
    "v_mov_b32 v42, 1\n"
    "s_mov_b64 exec, 1\n"
    "ds_add_u32 v98, v42 offset:8\n"
    "s_mov_b64 exec, -1\n"
    "s_waitcnt lgkmcnt(0)\n"
    "s_nop 7\n"
    ".Lwe_%=:\n"
    "s_nop 0\n"
    "s_branch .Lpe_%=\n"
    ".Lra_%=:\n"
    "s_setprio 3\n"
    "v_and_b32 v43, 63, v40\n"
    "v_and_b32 v124, 15, v40\n"
    "v_bfe_u32 v47, v40, 4, 2\n"
    "v_and_b32 v66, 48, v40\n"
    "v_lshlrev_b32 v10, 1, v124\n"
    "v_and_b32 v10, 28, v10\n"
    "v_lshl_add_u32 v10, v47, 6, v10\n"
    "v_lshlrev_b32 v2, 5, v47\n"
    "v_mul_u32_u24 v56, 12, v124\n"
    "s_mul_i32 s50, %[v], 192\n"
    "s_waitcnt lgkmcnt(0)\n"
    "s_mov_b32 s52, s72\n"
    "s_and_b32 s53, s73, 0xffff\n"
    "s_mov_b32 s54, 64\n"
    "s_mov_b32 s55, 0x20000\n"
    "s_load_dwordx2 s[70:71], s[70:71], 0x0\n"
    "buffer_load_dword v27, v10, s[52:55], 0 offen offset:0\n"
    "buffer_load_dword v126, v10, s[52:55], 0 offen offset:32\n"
    "s_mov_b32 s56, s76\n"
    "s_and_b32 s57, s77, 0xffff\n"
    "s_mov_b32 s58, 676\n"
    "s_mov_b32 s59, 0x20000\n"
    "buffer_load_dwordx4 v[48:51], v56, s[56:59], s50 offen offset:0\n"
    "s_mov_b32 s52, s74\n"
    "s_and_b32 s53, s75, 0xffff\n"
    "s_mov_b32 s54, 128\n"
    "s_mov_b32 s55, 0x20000\n"
    "buffer_load_dwordx4 v[128:131], v2, s[52:55], 0 offen offset:0\n"
    "buffer_load_dwordx4 v[140:143], v2, s[52:55], 0 offen offset:16\n"
    "s_movk_i32 s50, 371\n"
    "s_movk_i32 s51, 192\n"
    "v_add_u32 v136, 0, v124\n"
    "v_mul_u32_u24 v59, 37, v136\n"
    "v_lshrrev_b32 v59, 8, v59\n"
    "v_mad_u32_u24 v59, v59, s50, v136\n"
    "v_mad_u32_u24 v83, v59, s51, v66\n"
    "v_add_u32 v3, 16, v124\n"
    "v_mul_u32_u24 v46, 37, v3\n"
    "v_lshrrev_b32 v46, 8, v46\n"
    "v_mad_u32_u24 v46, v46, s50, v3\n"
    "v_mad_u32_u24 v84, v46, s51, v66\n"
    "v_add_u32 v151, 32, v124\n"
    "v_mul_u32_u24 v147, 37, v151\n"
    "v_lshrrev_b32 v147, 8, v147\n"
    "v_mad_u32_u24 v147, v147, s50, v151\n"
    "v_mad_u32_u24 v115, v147, s51, v66\n"
    "s_lshl_b32 s50, %[blk], 2\n"
    "s_add_i32 s50, s50, %[v]\n"
    "s_mul_i32 s51, %[blk], 12\n"
    "s_add_i32 s51, s51, %[v]\n"
    "s_sub_i32 s51, s51, 212\n"
    "s_cmp_lt_u32 %[blk], 27\n"
    "s_cselect_b32 s51, s50, s51\n"
    "s_cselect_b32 s60, 8, 12\n"
    "s_cselect_b32 s61, 4, 0\n"
    "s_sub_i32 s51, s51, 4\n"
    "s_mul_i32 s29, s51, 4947\n"
    "s_lshr_b32 s29, s29, 18\n"
    "s_mul_i32 s30, s29, 53\n"
    "s_sub_i32 s30, s51, s30\n"
    "s_sub_i32 s38, s50, 54\n"
    "s_min_u32 s38, s38, 53\n"
    "s_min_u32 s39, s50, 53\n"
    "s_add_i32 s50, s50, 2702\n"
    "s_lshr_b32 s31, %[v], 3\n"
    "s_cmp_lt_u32 %[v], s61\n"
    "s_cselect_b32 s29, s38, s29\n"
    "s_cselect_b32 s30, s39, s30\n"
    "s_cselect_b32 s51, s50, s51\n"
    "s_cselect_b32 s31, 2, s31\n"
    "s_cmp_lt_u32 %[v], s60\n"
    "s_cselect_b32 s51, s51, 9999\n"
    "s_cmp_lt_u32 s51, 2809\n"
    "s_cselect_b32 s31, s31, 3\n"
    "s_mul_i32 s28, s29, 2646\n"
    "s_mul_i32 s50, s30, 7\n"
    "s_add_i32 s28, s28, s50\n"
    "s_add_i32 s28, s28, 1137\n"
    "s_mul_i32 s28, s28, 192\n"
    "v_lshlrev_b32 v80, 2, v40\n"
    "v_add_u32 v80, 0x27c40, v80\n"
    "v_mov_b32 v148, 0\n"
    "s_cmp_eq_u32 %[v], 0\n"
    "s_cselect_b64 exec, 0xfff, 0\n"
    "ds_write_b32 v80, v148 offset:0\n"
    "s_mov_b64 exec, -1\n"
    "v_mov_b32 v1, 0x05040100\n"
    "v_mov_b32 v94, 0x07060302\n"
    "s_mov_b32 s60, 0xaaaaaaaa\n"
    "s_mov_b32 s61, 0xaaaaaaaa\n"
    "s_mov_b32 s48, 0xf0f0f0f0\n"
    "s_mov_b32 s49, 0xf0f0f0f0\n"
    "v_cndmask_b32 v162, v1, v94, s[60:61]\n"
    "v_cndmask_b32 v127, v1, v94, s[48:49]\n"
    "s_mov_b32 s48, 0xff00ff00\n"
    "s_mov_b32 s49, 0xff00ff00\n"
    "s_mul_i32 s50, %[v], 768\n"
    "s_add_i32 s50, s50, 122880\n"
    "v_mul_u32_u24 v119, 48, v124\n"
    "v_lshl_add_u32 v119, v47, 2, v119\n"
    "v_add_u32 v119, s50, v119\n"
    "v_mov_b32 v69, 0\n"
    "v_mov_b32 v163, 0\n"
    "v_mov_b32 v145, 0\n"
    "v_mov_b32 v15, 0\n"
    "s_mov_b64 s[96:97], s[64:65]\n"
    "s_mov_b64 s[98:99], s[68:69]\n"
    "s_add_u32 s24, s64, s28\n"
    "s_addc_u32 s25, s65, 0\n"
    "s_and_b32 s25, s25, 0xffff\n"
    "s_sub_i32 s18, 27433728, s28\n"
    "s_cmp_lt_u32 s31, 2\n"
    "s_cselect_b32 s26, s18, 0\n"
    "s_mov_b32 s27, 0x20000\n"
    "s_waitcnt vmcnt(3)\n"
    "v_perm_b32 v68, v126, v27, v162\n"
    "s_waitcnt vmcnt(2)\n"
    "v_mov_b32 v162, v48\n"
    "v_mov_b32 v144, v49\n"
    "v_mov_b32 v14, v50\n"
    "v_mfma_f32_16x16x16_bf16 v[76:79], v[68:69], v[162:163], 0\n"
    "v_mfma_f32_16x16x16_bf16 v[60:63], v[68:69], v[144:145], 0\n"
    "v_mfma_f32_16x16x16_bf16 v[148:151], v[68:69], v[14:15], 0\n"
    "s_waitcnt vmcnt(1)\n"
    "v_cndmask_b32 v128, v128, v129, s[48:49]\n"
    "v_cndmask_b32 v130, v130, v131, s[48:49]\n"
    "v_perm_b32 v22, v130, v128, v127\n"
    "s_waitcnt vmcnt(0)\n"
    "v_cndmask_b32 v140, v140, v141, s[48:49]\n"
    "v_cndmask_b32 v142, v142, v143, s[48:49]\n"
    "v_perm_b32 v23, v142, v140, v127\n"
    "v_max_f32 v76, 0, v76\n"
    "v_max_f32 v77, 0, v77\n"
    "v_max_f32 v78, 0, v78\n"
    "v_max_f32 v79, 0, v79\n"
    "v_cvt_pk_bf16_f32 v44, v76, v77\n"
    "v_cvt_pk_bf16_f32 v45, v78, v79\n"
    "v_max_f32 v60, 0, v60\n"
    "v_max_f32 v61, 0, v61\n"
    "v_max_f32 v62, 0, v62\n"
    "v_max_f32 v63, 0, v63\n"
    "v_cvt_pk_bf16_f32 v52, v60, v61\n"
    "v_cvt_pk_bf16_f32 v53, v62, v63\n"
    "v_max_f32 v148, 0, v148\n"
    "v_max_f32 v149, 0, v149\n"
    "v_max_f32 v150, 0, v150\n"
    "v_max_f32 v151, 0, v151\n"
    "v_cvt_pk_bf16_f32 v90, v148, v149\n"
    "v_cvt_pk_bf16_f32 v91, v150, v151\n"
    "v_mfma_f32_16x16x16_bf16 v[12:15], v[22:23], v[44:45], 0\n"
    "v_mfma_f32_16x16x16_bf16 v[136:139], v[22:23], v[52:53], 0\n"
    "v_mfma_f32_16x16x16_bf16 v[156:159], v[22:23], v[90:91], 0\n"
    "s_nop 5\n"
    "v_cvt_pk_bf16_f32 v12, v12, 0\n"
    "v_cvt_pk_bf16_f32 v136, v136, 0\n"
    "v_cvt_pk_bf16_f32 v156, v156, 0\n"
    "v_lshlrev_b32 v12, 16, v12\n"
    "v_lshlrev_b32 v136, 16, v136\n"
    "v_lshlrev_b32 v156, 16, v156\n"
    "v_mul_f32 v12, 0xbfb8aa3b, v12\n"
    "v_mul_f32 v136, 0xbfb8aa3b, v136\n"
    "v_mul_f32 v156, 0xbfb8aa3b, v156\n"
    "v_exp_f32 v12, v12\n"
    "v_exp_f32 v136, v136\n"
    "v_exp_f32 v156, v156\n"
    "v_add_f32 v12, 1.0, v12\n"
    "v_add_f32 v136, 1.0, v136\n"
    "v_add_f32 v156, 1.0, v156\n"
    "v_rcp_f32 v12, v12\n"
    "v_rcp_f32 v136, v136\n"
    "v_rcp_f32 v156, v156\n"
    "v_cvt_pk_bf16_f32 v12, v12, 0\n"
    "v_cvt_pk_bf16_f32 v136, v136, 0\n"
    "v_cvt_pk_bf16_f32 v156, v156, 0\n"
    "v_lshlrev_b32 v12, 16, v12\n"
    "v_lshlrev_b32 v136, 16, v136\n"
    "v_lshlrev_b32 v156, 16, v156\n"
    "s_mov_b32 s56, 0\n"
    "s_mov_b32 s57, -1\n"
    "s_waitcnt lgkmcnt(0)\n"
    "v_mov_b32 v8, s70\n"
    "v_mov_b32 v72, s71\n"
    "v_cndmask_b32 v8, v8, v72, s[56:57]\n"
    "s_mov_b32 s56, 0xffff0000\n"
    "s_mov_b32 s57, 0xffff0000\n"
    "v_lshlrev_b32 v72, 16, v8\n"
    "v_and_b32 v8, 0xffff0000, v8\n"
    "v_cndmask_b32 v8, v72, v8, s[56:57]\n"
    "v_max_f32 v8, 0xc2c80000, v8\n"
    "v_min_f32 v8, 0x40935dcc, v8\n"
    "v_cvt_pk_bf16_f32 v8, v8, 0\n"
    "v_lshlrev_b32 v8, 16, v8\n"
    "v_mul_f32 v8, 0x3fb8aa3b, v8\n"
    "v_exp_f32 v8, v8\n"
    "s_nop 0\n"
    "v_cvt_pk_bf16_f32 v8, v8, 0\n"
    "v_lshlrev_b32 v8, 16, v8\n"
    "v_mul_f32 v20, 0x3fb8aa3b, v8\n"
    "s_mov_b32 s50, 0x41e6d4ca\n"
    "s_nop 0\n"
    "v_readlane_b32 s44, v20, 0\n"
    "v_readlane_b32 s45, v20, 16\n"
    "v_readlane_b32 s46, v20, 32\n"
    "v_readlane_b32 s47, v20, 48\n"
    "v_cmp_nge_f32 s[56:57], s50, v20\n"
    "s_cmp_eq_u64 s[56:57], 0\n"
    "s_cselect_b32 s50, 0, 2\n"
    "s_max_u32 s31, s31, s50\n"
    "v_sub_f32 v72, 0xc1b8aa3b, v20\n"
    "v_fmamk_f32 v12, v12, 0x41b8aa3b, v72\n"
    "v_fmamk_f32 v136, v136, 0x41b8aa3b, v72\n"
    "v_fmamk_f32 v156, v156, 0x41b8aa3b, v72\n"
    "ds_write_b32 v119, v12 offset:0\n"
    "ds_write_b32 v119, v136 offset:16\n"
    "ds_write_b32 v119, v156 offset:32\n"
    "s_waitcnt lgkmcnt(0)\n"
    "s_barrier\n"
    "s_cmp_lt_u32 s31, 2\n"
    "s_cbranch_scc0 .Lnx_%=\n"
    "s_setprio 0\n"
    "buffer_load_dwordx4 v[0:3], v83, s[24:27], 0 offen offset:0 nt\n"
    "buffer_load_dwordx4 v[4:7], v83, s[24:27], 0 offen offset:64 nt\n"
    "buffer_load_dwordx4 v[8:11], v83, s[24:27], 0 offen offset:128 nt\n"
    "buffer_load_dwordx4 v[12:15], v84, s[24:27], 0 offen offset:0 nt\n"
    "buffer_load_dwordx4 v[16:19], v84, s[24:27], 0 offen offset:64 nt\n"
    "buffer_load_dwordx4 v[20:23], v84, s[24:27], 0 offen offset:128 nt\n"
    "buffer_load_dwordx4 v[24:27], v115, s[24:27], 0 offen offset:0 nt\n"
    "buffer_load_dwordx4 v[28:31], v115, s[24:27], 0 offen offset:64 nt\n"
    "buffer_load_dwordx4 v[32:35], v115, s[24:27], 0 offen offset:128 nt\n"
    "s_setprio 3\n"
    ".Lnx_%=:\n"
    "s_cmp_eq_u32 %[v], 0\n"
    "s_cbranch_scc0 .Lct_%=\n"
    "v_lshrrev_b32 v95, 1, v124\n"
    "v_and_b32 v95, 4, v95\n"
    "v_xor_b32 v95, v95, v124\n"
    "v_xor_b32 v95, v95, v47\n"
    "v_add_u32 v95, v95, v66\n"
    "v_lshlrev_b32 v50, 4, v95\n"
    "v_and_b32 v95, 1, v124\n"
    "v_lshlrev_b32 v95, 4, v95\n"
    "v_mov_b32 v97, 0x3f80\n"
    "v_lshlrev_b32 v95, v95, v97\n"
    "s_mov_b32 s60, 0xcccccccc\n"
    "s_mov_b32 s61, 0xcccccccc\n"
    "v_cndmask_b32 v144, v95, 0, s[60:61]\n"
    "v_cndmask_b32 v145, 0, v95, s[60:61]\n"
    "v_mov_b32 v146, v83\n"
    "v_mov_b32 v147, v84\n"
    "v_cndmask_b32 v76, 0, -1, s[60:61]\n"
    "v_lshlrev_b32 v77, 2, v124\n"
    "v_sub_u32 v77, v77, v66\n"
    "v_add_u32 v77, 1024, v77\n"
    "v_mov_b32 v78, 0x3f803f80\n"
    "s_mov_b32 s60, 0xaaaaaaaa\n"
    "s_mov_b32 s61, 0xaaaaaaaa\n"
    "v_cndmask_b32 v78, v78, 0, s[60:61]\n"
    "v_mov_b32 v79, v115\n"
    "ds_write_b128 v50, v[144:147] offset:55296\n"
    "ds_write_b128 v50, v[76:79] offset:56320\n"
    "v_mov_b32 v104, 0\n"
    "v_mov_b32 v105, 0\n"
    "v_mov_b32 v106, 0\n"
    "v_mov_b32 v107, 0\n"
    "s_movk_i32 s50, 1472\n"
    "v_min_u32 v95, 11, v124\n"
    "v_mad_u32_u24 v95, v95, s50, v66\n"
    "v_add_u32 v95, 0x1f180, v95\n"
    "ds_write_b128 v95, v[104:107] offset:0\n"
    "v_mov_b32 v46, 0x27c40\n"
    "v_mov_b32 v165, 1\n"
    "s_mov_b64 exec, 1\n"
    "ds_add_u32 v46, v165 offset:16\n"
    "s_mov_b64 exec, -1\n"
    ".Lct_%=:\n"
    "s_add_u32 s16, s94, s28\n"
    "s_addc_u32 s17, s95, 0\n"
    "s_and_b32 s17, s17, 0xffff\n"
    "s_mov_b32 s19, 0x20000\n"
    "s_mul_i32 s50, s29, 54\n"
    "s_add_i32 s50, s50, s30\n"
    "s_mul_i32 s50, s50, 4802\n"
    "s_add_u32 s38, s92, s50\n"
    "s_addc_u32 s39, s93, 0\n"
    "s_sub_u32 s20, s38, 57344\n"
    "s_subb_u32 s21, s39, 0\n"
    "s_and_b32 s21, s21, 0xffff\n"
    "s_mov_b32 s22, 62146\n"
    "s_mov_b32 s23, 0x20000\n"
    "s_cmp_eq_u32 s29, 53\n"
    "s_cselect_b32 s29, 4, 99\n"
    "s_cmp_eq_u32 s30, 53\n"
    "s_cselect_b32 s30, 4, 99\n"
    "s_and_b32 s34, %[v], 3\n"
    "s_mul_i32 s34, s34, 4816\n"
    "s_add_i32 s34, s34, 143616\n"
    "s_movk_i32 s37, 4096\n"
    "s_mov_b32 s40, 0x2a0cbccc\n"
    "s_mov_b32 s41, 0x2a0cbccc\n"
    "s_mov_b32 s42, 0x387ba882\n"
    "s_mov_b32 s43, 0x387ba882\n"
    "v_lshrrev_b32 v152, 1, v124\n"
    "v_and_b32 v152, 4, v152\n"
    "v_xor_b32 v152, v152, v124\n"
    "v_xor_b32 v152, v152, v47\n"
    "v_add_u32 v152, v152, v66\n"
    "v_lshlrev_b32 v36, 4, v152\n"
    "v_lshlrev_b32 v37, 4, v43\n"
    "v_add_u32 v37, 0xe000, v37\n"
    "s_mul_i32 s50, %[v], 1472\n"
    "s_add_i32 s50, s50, 125952\n"
    "v_add_u32 v38, s50, v66\n"
    "s_nop 0\n"
    ".Lpe_%=:\n"
    "s_cmp_lt_u32 s31, 2\n"
    "s_cbranch_scc0 .Lslow_%=\n"
    ".Lwin_%=:\n"
    "s_waitcnt lgkmcnt(0)\n"
    "s_cmp_eq_u32 s31, 2\n"
    "s_cselect_b64 vcc, -1, 0\n"
    "s_mov_b32 s35, 0\n"
    "s_cmp_eq_u32 s31, 3\n"
    "s_cbranch_scc1 .Ltails_%=\n"
    "s_mov_b32 s52, 0x10001\n"
    "s_mov_b32 s53, 0x10001\n"
    "s_mov_b32 s54, 0x20002\n"
    "s_mov_b32 s55, 0x20002\n"
    "v_mov_b32 v68, 0x27c40\n"
    "ds_read_b32 v61, v68 offset:16\n"
    "s_waitcnt lgkmcnt(0)\n"
    "v_readfirstlane_b32 s33, v61\n"
    "s_cmp_ge_i32 s33, 5\n"
    "s_cbranch_scc1 .Ldv_%=\n"
    ".Llv_%=:\n"
    "s_sleep 2\n"
    "ds_read_b32 v61, v68 offset:16\n"
    "s_waitcnt lgkmcnt(0)\n"
    "v_readfirstlane_b32 s33, v61\n"
    "s_cmp_ge_i32 s33, 5\n"
    "s_cbranch_scc0 .Llv_%=\n"
    ".Ldv_%=:\n"
    "s_waitcnt vmcnt(0)\n"
    "ds_read_b128 v[88:91], v36 offset:1024\n"
    "ds_read_b128 v[124:127], v36 offset:0\n"
    "s_setprio 1\n"
    "s_cmp_eq_u32 s31, 1\n"
    "s_cbranch_scc0 .Lma_%=\n"
    "buffer_load_dwordx4 v[112:115], v37, s[20:23], 0 offen offset:0 nt\n"
    "buffer_load_dwordx4 v[164:167], v37, s[20:23], 0 offen offset:1024 nt\n"
    "buffer_load_dwordx4 v[120:123], v37, s[20:23], 0 offen offset:2048 nt\n"
    "buffer_load_dwordx4 v[72:75], v37, s[20:23], 0 offen offset:3072 nt\n"
    "v_min_u32 v116, 0xe2b2, v37\n"
    "buffer_load_dwordx4 v[116:119], v116, s[20:23], s37 offen nt\n"
    ".Lma_%=:\n"
    "s_waitcnt lgkmcnt(0)\n"
    "v_mfma_f32_16x16x32_bf16 v[152:155], v[124:127], v[0:3], 0\n"
    "ds_read_b128 v[160:163], v36 offset:2048\n"
    "ds_read_b128 v[52:55], v36 offset:56320\n"
    "v_mfma_f32_16x16x32_bf16 v[152:155], v[88:91], v[4:7], v[152:155]\n"
    "ds_read_b128 v[64:67], v36 offset:3072\n"
    "ds_read_b128 v[96:99], v36 offset:4096\n"
    "s_waitcnt lgkmcnt(3)\n"
    "v_mfma_f32_16x16x32_bf16 v[152:155], v[160:163], v[8:11], v[152:155]\n"
    "ds_read_b128 v[48:51], v36 offset:5120\n"
    "v_mfma_f32_16x16x32_bf16 v[104:107], v[124:127], v[12:15], 0\n"
    "s_waitcnt lgkmcnt(3)\n"
    "v_mov_b32 v55, 0x27c40\n"
    "v_mfma_f32_16x16x32_bf16 v[104:107], v[88:91], v[16:19], v[104:107]\n"
    "v_add_u32 v53, v38, v53\n"
    "v_mfma_f32_16x16x32_bf16 v[104:107], v[160:163], v[20:23], v[104:107]\n"
    "ds_read_b128 v[92:95], v36 offset:6144\n"
    "ds_read_b128 v[76:79], v36 offset:7168\n"
    "v_mfma_f32_16x16x32_bf16 v[40:43], v[124:127], v[24:27], 0\n"
    "v_mfma_f32_16x16x32_bf16 v[40:43], v[88:91], v[28:31], v[40:43]\n"
    "ds_read_b128 v[156:159], v36 offset:8192\n"
    "v_mfma_f32_16x16x32_bf16 v[40:43], v[160:163], v[32:35], v[40:43]\n"
    "s_waitcnt lgkmcnt(4)\n"
    "v_mfma_f32_16x16x32_bf16 v[108:111], v[64:67], v[0:3], 0\n"
    "v_mfma_f32_16x16x32_bf16 v[108:111], v[96:99], v[4:7], v[108:111]\n"
    "s_waitcnt lgkmcnt(3)\n"
    "v_fmaak_f32 v124, v152, v152, 0x2a8cbccc\n"
    "v_fmac_f32 v124, v153, v153\n"
    "v_mfma_f32_16x16x32_bf16 v[108:111], v[48:51], v[8:11], v[108:111]\n"
    "v_fmac_f32 v124, v154, v154\n"
    "v_fmac_f32 v124, v155, v155\n"
    "v_mfma_f32_16x16x32_bf16 v[88:91], v[64:67], v[12:15], 0\n"
    "v_fmaak_f32 v102, v104, v104, 0x2a8cbccc\n"
    "v_fmac_f32 v102, v105, v105\n"
    "v_mfma_f32_16x16x32_bf16 v[88:91], v[96:99], v[16:19], v[88:91]\n"
    "v_fmac_f32 v102, v106, v106\n"
    "v_fmac_f32 v102, v107, v107\n"
    "v_mfma_f32_16x16x32_bf16 v[88:91], v[48:51], v[20:23], v[88:91]\n"
    "ds_read_b128 v[132:135], v36 offset:39936\n"
    "v_fmaak_f32 v125, v40, v40, 0x2a8cbccc\n"
    "v_fmac_f32 v125, v41, v41\n"
    "v_mfma_f32_16x16x32_bf16 v[136:139], v[64:67], v[24:27], 0\n"
    "ds_read_b128 v[44:47], v36 offset:40960\n"
    "v_fmac_f32 v125, v42, v42\n"
    "v_fmac_f32 v125, v43, v43\n"
    "v_mfma_f32_16x16x32_bf16 v[136:139], v[96:99], v[28:31], v[136:139]\n"
    "ds_read_b128 v[140:143], v36 offset:41984\n"
    "v_mfma_f32_16x16x32_bf16 v[136:139], v[48:51], v[32:35], v[136:139]\n"
    "s_waitcnt lgkmcnt(3)\n"
    "v_mfma_f32_16x16x32_bf16 v[96:99], v[92:95], v[0:3], 0\n"
    "v_mfma_f32_16x16x32_bf16 v[96:99], v[76:79], v[4:7], v[96:99]\n"
    "v_fmac_f32 v124, v108, v108\n"
    "v_fmac_f32 v124, v109, v109\n"
    "v_mfma_f32_16x16x32_bf16 v[96:99], v[156:159], v[8:11], v[96:99]\n"
    "v_fmaak_f32 v60, v110, v110, 0x2a8cbccc\n"
    "v_fmac_f32 v60, v111, v111\n"
    "v_mfma_f32_16x16x32_bf16 v[160:163], v[92:95], v[12:15], 0\n"
    "v_fmac_f32 v102, v88, v88\n"
    "v_fmac_f32 v102, v89, v89\n"
    "v_mfma_f32_16x16x32_bf16 v[160:163], v[76:79], v[16:19], v[160:163]\n"
    "v_fmaak_f32 v128, v90, v90, 0x2a8cbccc\n"
    "v_fmac_f32 v128, v91, v91\n"
    "v_mfma_f32_16x16x32_bf16 v[160:163], v[156:159], v[20:23], v[160:163]\n"
    "v_fmac_f32 v125, v136, v136\n"
    "v_fmac_f32 v125, v137, v137\n"
    "v_mfma_f32_16x16x32_bf16 v[64:67], v[92:95], v[24:27], 0\n"
    "v_fmaak_f32 v126, v138, v138, 0x2a8cbccc\n"
    "v_fmac_f32 v126, v139, v139\n"
    "v_mfma_f32_16x16x32_bf16 v[64:67], v[76:79], v[28:31], v[64:67]\n"
    "ds_read_b128 v[48:51], v36 offset:36864\n"
    "v_mfma_f32_16x16x32_bf16 v[64:67], v[156:159], v[32:35], v[64:67]\n"
    "s_waitcnt lgkmcnt(1)\n"
    "ds_read_b128 v[156:159], v36 offset:37888\n"
    "v_mfma_f32_16x16x32_bf16 v[144:147], v[0:3], v[132:135], 0\n"
    "ds_read_b64 v[62:63], v36 offset:55296\n"
    "ds_read_b128 v[68:71], v36 offset:38912\n"
    "v_mfma_f32_16x16x32_bf16 v[144:147], v[4:7], v[44:47], v[144:147]\n"
    "v_fmac_f32 v60, v96, v96\n"
    "v_fmac_f32 v60, v97, v97\n"
    "v_mfma_f32_16x16x32_bf16 v[144:147], v[8:11], v[140:143], v[144:147]\n"
    "v_fmac_f32 v60, v98, v98\n"
    "v_fmac_f32 v60, v99, v99\n"
    "v_mfma_f32_16x16x32_bf16 v[76:79], v[12:15], v[132:135], 0\n"
    "v_fmac_f32 v128, v160, v160\n"
    "v_fmac_f32 v128, v161, v161\n"
    "v_mfma_f32_16x16x32_bf16 v[76:79], v[16:19], v[44:47], v[76:79]\n"
    "v_fmac_f32 v128, v162, v162\n"
    "v_fmac_f32 v128, v163, v163\n"
    "v_mfma_f32_16x16x32_bf16 v[76:79], v[20:23], v[140:143], v[76:79]\n"
    "v_fmac_f32 v126, v64, v64\n"
    "v_fmac_f32 v126, v65, v65\n"
    "v_mfma_f32_16x16x32_bf16 v[92:95], v[24:27], v[132:135], 0\n"
    "v_fmac_f32 v126, v66, v66\n"
    "v_fmac_f32 v126, v67, v67\n"
    "v_mfma_f32_16x16x32_bf16 v[92:95], v[28:31], v[44:47], v[92:95]\n"
    "v_cvt_pk_bf16_f32 v60, v124, v60\n"
    "v_cvt_pk_bf16_f32 v61, v102, v128\n"
    "v_mfma_f32_16x16x32_bf16 v[92:95], v[32:35], v[140:143], v[92:95]\n"
    "s_waitcnt lgkmcnt(0)\n"
    "ds_read_b128 v[80:83], v36 offset:43008\n"
    "v_cvt_pk_bf16_f32 v44, v125, v126\n"
    "v_cvt_pk_bf16_f32 v45, v125, v126\n"
    "v_mfma_f32_16x16x16_bf16 v[132:135], v[62:63], v[60:61], 0\n"
    "ds_read_b128 v[124:127], v36 offset:44032\n"
    "v_cvt_pk_bf16_f32 v140, v144, v145\n"
    "v_cvt_pk_bf16_f32 v141, v146, v147\n"
    "v_mfma_f32_16x16x16_bf16 v[100:103], v[62:63], v[44:45], 0\n"
    "ds_read_b128 v[128:131], v36 offset:45056\n"
    "v_cvt_pk_bf16_f32 v142, v76, v77\n"
    "v_cvt_pk_bf16_f32 v143, v78, v79\n"
    "v_mfma_f32_16x16x32_bf16 v[44:47], v[0:3], v[48:51], 0\n"
    "v_cvt_pk_bf16_f32 v76, v92, v93\n"
    "v_cvt_pk_bf16_f32 v77, v94, v95\n"
    "v_mfma_f32_16x16x32_bf16 v[44:47], v[4:7], v[156:159], v[44:47]\n"
    "v_bitop3_b32 v92, v52, v54, v140 bitop3:0xca\n"
    "v_bitop3_b32 v140, v52, v140, v54 bitop3:0xca\n"
    "v_mfma_f32_16x16x32_bf16 v[44:47], v[8:11], v[68:71], v[44:47]\n"
    "v_bitop3_b32 v93, v52, v54, v141 bitop3:0xca\n"
    "v_bitop3_b32 v141, v52, v141, v54 bitop3:0xca\n"
    "v_mfma_f32_16x16x32_bf16 v[84:87], v[12:15], v[48:51], 0\n"
    "v_rsq_f32 v132, v132\n"
    "v_mfma_f32_16x16x32_bf16 v[84:87], v[16:19], v[156:159], v[84:87]\n"
    "v_rsq_f32 v133, v133\n"
    "v_mfma_f32_16x16x32_bf16 v[84:87], v[20:23], v[68:71], v[84:87]\n"
    "v_rsq_f32 v134, v134\n"
    "v_mfma_f32_16x16x32_bf16 v[56:59], v[24:27], v[48:51], 0\n"
    "v_rsq_f32 v135, v135\n"
    "v_mfma_f32_16x16x32_bf16 v[56:59], v[28:31], v[156:159], v[56:59]\n"
    "v_rsq_f32 v100, v100\n"
    "v_mfma_f32_16x16x32_bf16 v[56:59], v[32:35], v[68:71], v[56:59]\n"
    "s_waitcnt lgkmcnt(0)\n"
    "v_rsq_f32 v101, v101\n"
    "v_mfma_f32_16x16x32_bf16 v[68:71], v[0:3], v[80:83], 0\n"
    "v_mul_f32 v132, s44, v132\n"
    "v_mul_f32 v102, s45, v133\n"
    "v_mfma_f32_16x16x32_bf16 v[68:71], v[4:7], v[124:127], v[68:71]\n"
    "v_mul_f32 v134, s44, v134\n"
    "v_mul_f32 v158, s45, v135\n"
    "v_mfma_f32_16x16x32_bf16 v[68:71], v[8:11], v[128:131], v[68:71]\n"
    "ds_read_b128 v[144:147], v36 offset:18432\n"
    "v_mul_f32 v100, s44, v100\n"
    "v_mul_f32 v78, s45, v101\n"
    "v_mfma_f32_16x16x32_bf16 v[60:63], v[12:15], v[80:83], 0\n"
    "ds_read_b128 v[48:51], v36 offset:19456\n"
    "v_cvt_pk_bf16_f32 v148, v44, v45\n"
    "v_cvt_pk_bf16_f32 v149, v46, v47\n"
    "v_mfma_f32_16x16x32_bf16 v[60:63], v[16:19], v[124:127], v[60:63]\n"
    "ds_read_b128 v[44:47], v36 offset:20480\n"
    "v_cvt_pk_bf16_f32 v150, v84, v85\n"
    "v_cvt_pk_bf16_f32 v151, v86, v87\n"
    "v_mfma_f32_16x16x32_bf16 v[60:63], v[20:23], v[128:131], v[60:63]\n"
    "v_cvt_pk_bf16_f32 v156, v56, v57\n"
    "v_cvt_pk_bf16_f32 v157, v58, v59\n"
    "v_mfma_f32_16x16x32_bf16 v[56:59], v[24:27], v[80:83], 0\n"
    "v_bitop3_b32 v94, v52, v54, v142 bitop3:0xca\n"
    "v_bitop3_b32 v142, v52, v142, v54 bitop3:0xca\n"
    "v_mfma_f32_16x16x32_bf16 v[56:59], v[28:31], v[124:127], v[56:59]\n"
    "v_bitop3_b32 v95, v52, v54, v143 bitop3:0xca\n"
    "v_bitop3_b32 v143, v52, v143, v54 bitop3:0xca\n"
    "v_mfma_f32_16x16x32_bf16 v[56:59], v[32:35], v[128:131], v[56:59]\n"
    "ds_read_b128 v[124:127], v36 offset:22528\n"
    "v_bitop3_b32 v128, v52, v54, v76 bitop3:0xca\n"
    "v_bitop3_b32 v76, v52, v76, v54 bitop3:0xca\n"
    "v_bitop3_b32 v129, v52, v54, v77 bitop3:0xca\n"
    "v_bitop3_b32 v77, v52, v77, v54 bitop3:0xca\n"
    "s_setprio 3\n"
    "v_pk_mul_f32 v[152:153], v[152:153], v[132:133] op_sel_hi:[1,0]\n"
    "v_pk_mul_f32 v[154:155], v[154:155], v[132:133] op_sel_hi:[1,0]\n"
    "v_pk_mul_f32 v[108:109], v[108:109], v[132:133] op_sel_hi:[1,0]\n"
    "v_pk_mul_f32 v[110:111], v[110:111], v[102:103] op_sel_hi:[1,0]\n"
    "v_pk_mul_f32 v[96:97], v[96:97], v[102:103] op_sel_hi:[1,0]\n"
    "v_pk_mul_f32 v[98:99], v[98:99], v[102:103] op_sel_hi:[1,0]\n"
    "v_pk_mul_f32 v[104:105], v[104:105], v[134:135] op_sel_hi:[1,0]\n"
    "v_pk_mul_f32 v[106:107], v[106:107], v[134:135] op_sel_hi:[1,0]\n"
    "v_pk_mul_f32 v[88:89], v[88:89], v[134:135] op_sel_hi:[1,0]\n"
    "v_pk_mul_f32 v[90:91], v[90:91], v[158:159] op_sel_hi:[1,0]\n"
    "v_pk_mul_f32 v[160:161], v[160:161], v[158:159] op_sel_hi:[1,0]\n"
    "v_pk_mul_f32 v[162:163], v[162:163], v[158:159] op_sel_hi:[1,0]\n"
    "v_pk_mul_f32 v[40:41], v[40:41], v[100:101] op_sel_hi:[1,0]\n"
    "v_pk_mul_f32 v[42:43], v[42:43], v[100:101] op_sel_hi:[1,0]\n"
    "v_pk_mul_f32 v[136:137], v[136:137], v[100:101] op_sel_hi:[1,0]\n"
    "v_pk_mul_f32 v[138:139], v[138:139], v[78:79] op_sel_hi:[1,0]\n"
    "v_pk_mul_f32 v[64:65], v[64:65], v[78:79] op_sel_hi:[1,0]\n"
    "v_pk_mul_f32 v[66:67], v[66:67], v[78:79] op_sel_hi:[1,0]\n"
    "s_setprio 1\n"
    "s_waitcnt lgkmcnt(0)\n"
    "v_mfma_f32_16x16x32_bf16 v[100:103], v[144:147], v[0:3], 0\n"
    "ds_read_b128 v[132:135], v36 offset:21504\n"
    "ds_read_b128 v[78:81], v36 offset:23552\n"
    "v_cvt_pk_bf16_f32 v82, v152, v153\n"
    "v_cvt_pk_bf16_f32 v83, v154, v155\n"
    "v_mfma_f32_16x16x32_bf16 v[100:103], v[48:51], v[4:7], v[100:103]\n"
    "v_cvt_pk_bf16_f32 v84, v108, v109\n"
    "v_cvt_pk_bf16_f32 v85, v110, v111\n"
    "v_mfma_f32_16x16x32_bf16 v[100:103], v[44:47], v[8:11], v[100:103]\n"
    "v_cvt_pk_bf16_f32 v86, v96, v97\n"
    "v_cvt_pk_bf16_f32 v87, v98, v99\n"
    "v_mfma_f32_16x16x32_bf16 v[152:155], v[144:147], v[12:15], 0\n"
    "v_cvt_pk_bf16_f32 v104, v104, v105\n"
    "v_cvt_pk_bf16_f32 v105, v106, v107\n"
    "v_mfma_f32_16x16x32_bf16 v[152:155], v[48:51], v[16:19], v[152:155]\n"
    "v_cvt_pk_bf16_f32 v106, v88, v89\n"
    "v_cvt_pk_bf16_f32 v107, v90, v91\n"
    "v_mfma_f32_16x16x32_bf16 v[152:155], v[44:47], v[20:23], v[152:155]\n"
    "v_cvt_pk_bf16_f32 v108, v160, v161\n"
    "v_cvt_pk_bf16_f32 v109, v162, v163\n"
    "v_mfma_f32_16x16x32_bf16 v[160:163], v[144:147], v[24:27], 0\n"
    "ds_read_b128 v[96:99], v36 offset:24576\n"
    "ds_read_b128 v[144:147], v36 offset:25600\n"
    "v_cvt_pk_bf16_f32 v40, v40, v41\n"
    "v_cvt_pk_bf16_f32 v41, v42, v43\n"
    "v_mfma_f32_16x16x32_bf16 v[160:163], v[48:51], v[28:31], v[160:163]\n"
    "v_cvt_pk_bf16_f32 v42, v136, v137\n"
    "v_cvt_pk_bf16_f32 v43, v138, v139\n"
    "v_mfma_f32_16x16x32_bf16 v[160:163], v[44:47], v[32:35], v[160:163]\n"
    "s_waitcnt lgkmcnt(2)\n"
    "ds_read_b128 v[136:139], v36 offset:26624\n"
    "v_cvt_pk_bf16_f32 v44, v64, v65\n"
    "v_cvt_pk_bf16_f32 v45, v66, v67\n"
    "v_mfma_f32_16x16x32_bf16 v[88:91], v[132:135], v[0:3], 0\n"
    "v_cvt_pk_bf16_f32 v64, v68, v69\n"
    "v_cvt_pk_bf16_f32 v65, v70, v71\n"
    "v_mfma_f32_16x16x32_bf16 v[88:91], v[124:127], v[4:7], v[88:91]\n"
    "v_cvt_pk_bf16_f32 v66, v60, v61\n"
    "v_cvt_pk_bf16_f32 v67, v62, v63\n"
    "v_mfma_f32_16x16x32_bf16 v[88:91], v[78:81], v[8:11], v[88:91]\n"
    "v_cvt_pk_bf16_f32 v68, v56, v57\n"
    "v_cvt_pk_bf16_f32 v69, v58, v59\n"
    "v_mfma_f32_16x16x32_bf16 v[48:51], v[132:135], v[12:15], 0\n"
    "v_fmaak_f32 v159, v100, v100, 0x2a8cbccc\n"
    "v_fmac_f32 v159, v101, v101\n"
    "v_mfma_f32_16x16x32_bf16 v[48:51], v[124:127], v[16:19], v[48:51]\n"
    "v_fmac_f32 v159, v102, v102\n"
    "v_fmac_f32 v159, v103, v103\n"
    "v_mfma_f32_16x16x32_bf16 v[48:51], v[78:81], v[20:23], v[48:51]\n"
    "v_fmaak_f32 v47, v152, v152, 0x2a8cbccc\n"
    "v_fmac_f32 v47, v153, v153\n"
    "v_mfma_f32_16x16x32_bf16 v[60:63], v[132:135], v[24:27], 0\n"
    "v_fmac_f32 v47, v154, v154\n"
    "v_fmac_f32 v47, v155, v155\n"
    "v_mfma_f32_16x16x32_bf16 v[60:63], v[124:127], v[28:31], v[60:63]\n"
    "v_fmaak_f32 v46, v160, v160, 0x2a8cbccc\n"
    "v_fmac_f32 v46, v161, v161\n"
    "v_mfma_f32_16x16x32_bf16 v[60:63], v[78:81], v[32:35], v[60:63]\n"
    "s_waitcnt lgkmcnt(0)\n"
    "v_fmac_f32 v46, v162, v162\n"
    "v_fmac_f32 v46, v163, v163\n"
    "v_mfma_f32_16x16x32_bf16 v[56:59], v[96:99], v[0:3], 0\n"
    "v_mfma_f32_16x16x32_bf16 v[56:59], v[144:147], v[4:7], v[56:59]\n"
    "v_fmac_f32 v159, v88, v88\n"
    "v_fmac_f32 v159, v89, v89\n"
    "v_mfma_f32_16x16x32_bf16 v[56:59], v[136:139], v[8:11], v[56:59]\n"
    "ds_read_b32 v111, v55 offset:0\n"
    "v_fmaak_f32 v80, v90, v90, 0x2a8cbccc\n"
    "v_fmac_f32 v80, v91, v91\n"
    "v_mfma_f32_16x16x32_bf16 v[124:127], v[96:99], v[12:15], 0\n"
    "v_fmac_f32 v47, v48, v48\n"
    "v_fmac_f32 v47, v49, v49\n"
    "v_mfma_f32_16x16x32_bf16 v[124:127], v[144:147], v[16:19], v[124:127]\n"
    "v_fmaak_f32 v110, v50, v50, 0x2a8cbccc\n"
    "v_fmac_f32 v110, v51, v51\n"
    "v_mfma_f32_16x16x32_bf16 v[124:127], v[136:139], v[20:23], v[124:127]\n"
    "ds_read_b64 v[78:79], v36 offset:55296\n"
    "v_fmac_f32 v46, v60, v60\n"
    "v_fmac_f32 v46, v61, v61\n"
    "v_mfma_f32_16x16x32_bf16 v[132:135], v[96:99], v[24:27], 0\n"
    "v_fmaak_f32 v81, v62, v62, 0x2a8cbccc\n"
    "v_fmac_f32 v81, v63, v63\n"
    "v_mfma_f32_16x16x32_bf16 v[132:135], v[144:147], v[28:31], v[132:135]\n"
    "v_mfma_f32_16x16x32_bf16 v[132:135], v[136:139], v[32:35], v[132:135]\n"
    "s_cmp_eq_u32 s31, 1\n"
    "s_cbranch_scc0 .Lfa_%=\n"
    "s_waitcnt vmcnt(0)\n"
    "v_bitop3_b32 v112, v112, v113, v114 bitop3:0xfe\n"
    "v_bitop3_b32 v112, v112, v115, v164 bitop3:0xfe\n"
    "v_bitop3_b32 v112, v112, v165, v166 bitop3:0xfe\n"
    "v_bitop3_b32 v112, v112, v167, v120 bitop3:0xfe\n"
    "v_bitop3_b32 v112, v112, v121, v122 bitop3:0xfe\n"
    "v_bitop3_b32 v112, v112, v123, v72 bitop3:0xfe\n"
    "v_bitop3_b32 v112, v112, v73, v74 bitop3:0xfe\n"
    "v_bitop3_b32 v112, v112, v75, v116 bitop3:0xfe\n"
    "v_bitop3_b32 v112, v112, v117, v118 bitop3:0xfe\n"
    "v_or_b32 v112, v112, v119\n"
    "v_and_b32 v112, 0x7fff7fff, v112\n"
    "v_cmp_ne_u32 s[48:49], 0, v112\n"
    "s_cmp_lg_u64 s[48:49], 0\n"
    "s_cselect_b32 s35, 1, s35\n"
    ".Lfa_%=:\n"
    "v_mov_b32 v147, 0\n"
    "v_fmac_f32 v80, v56, v56\n"
    "v_fmac_f32 v80, v57, v57\n"
    "v_fmac_f32 v80, v58, v58\n"
    "v_fmac_f32 v80, v59, v59\n"
    "v_fmac_f32 v110, v124, v124\n"
    "v_fmac_f32 v110, v125, v125\n"
    "v_fmac_f32 v110, v126, v126\n"
    "v_fmac_f32 v110, v127, v127\n"
    "v_fmac_f32 v81, v132, v132\n"
    "v_fmac_f32 v81, v133, v133\n"
    "v_fmac_f32 v81, v134, v134\n"
    "v_fmac_f32 v81, v135, v135\n"
    "v_cvt_pk_bf16_f32 v164, v159, v80\n"
    "v_cvt_pk_bf16_f32 v165, v47, v110\n"
    "v_cvt_pk_bf16_f32 v98, v46, v81\n"
    "v_cvt_pk_bf16_f32 v99, v46, v81\n"
    "s_waitcnt lgkmcnt(0)\n"
    "v_mfma_f32_16x16x16_bf16 v[120:123], v[78:79], v[164:165], 0\n"
    "v_mfma_f32_16x16x16_bf16 v[136:139], v[78:79], v[98:99], 0\n"
    "v_readfirstlane_b32 s33, v111\n"
    "s_cmp_ge_i32 s33, 4\n"
    "s_cbranch_scc1 .Lda_%=\n"
    ".Lla_%=:\n"
    "s_sleep 2\n"
    "ds_read_b32 v111, v55 offset:0\n"
    "s_waitcnt lgkmcnt(0)\n"
    "v_readfirstlane_b32 s33, v111\n"
    "s_cmp_ge_i32 s33, 4\n"
    "s_cbranch_scc0 .Lla_%=\n"
    ".Lda_%=:\n"
    "ds_read2_b32 v[70:71], v53 offset0:32 offset1:32\n"
    "ds_read2_b32 v[158:159], v53 offset0:0 offset1:0\n"
    "ds_read_b32 v131, v53 offset:64\n"
    "ds_read2_b32 v[78:79], v53 offset0:16 offset1:16\n"
    "s_nop 1\n"
    "v_rsq_f32 v120, v120\n"
    "v_rsq_f32 v80, v121\n"
    "v_rsq_f32 v122, v122\n"
    "v_rsq_f32 v46, v123\n"
    "v_rsq_f32 v136, v136\n"
    "v_rsq_f32 v110, v137\n"
    "ds_read_b128 v[96:99], v38 offset:1408\n"
    "s_mov_b64 exec, s[52:53]\n"
    "ds_read_b128 v[96:99], v38 offset:256\n"
    "s_mov_b64 exec, -1\n"
    "ds_read_b128 v[72:75], v37 offset:59392\n"
    "ds_read_b128 v[112:115], v37 offset:60416\n"
    "s_setprio 3\n"
    "v_pk_mul_f32 v[100:101], v[100:101], v[120:121] op_sel_hi:[1,0]\n"
    "v_pk_mul_f32 v[102:103], v[102:103], v[120:121] op_sel_hi:[1,0]\n"
    "v_pk_mul_f32 v[88:89], v[88:89], v[120:121] op_sel_hi:[1,0]\n"
    "v_pk_mul_f32 v[90:91], v[90:91], v[80:81] op_sel_hi:[1,0]\n"
    "v_pk_mul_f32 v[56:57], v[56:57], v[80:81] op_sel_hi:[1,0]\n"
    "v_pk_mul_f32 v[58:59], v[58:59], v[80:81] op_sel_hi:[1,0]\n"
    "v_pk_mul_f32 v[152:153], v[152:153], v[122:123] op_sel_hi:[1,0]\n"
    "v_pk_mul_f32 v[154:155], v[154:155], v[122:123] op_sel_hi:[1,0]\n"
    "v_pk_mul_f32 v[48:49], v[48:49], v[122:123] op_sel_hi:[1,0]\n"
    "v_pk_mul_f32 v[50:51], v[50:51], v[46:47] op_sel_hi:[1,0]\n"
    "v_pk_mul_f32 v[124:125], v[124:125], v[46:47] op_sel_hi:[1,0]\n"
    "v_pk_mul_f32 v[126:127], v[126:127], v[46:47] op_sel_hi:[1,0]\n"
    "v_pk_mul_f32 v[160:161], v[160:161], v[136:137] op_sel_hi:[1,0]\n"
    "v_pk_mul_f32 v[162:163], v[162:163], v[136:137] op_sel_hi:[1,0]\n"
    "v_pk_mul_f32 v[60:61], v[60:61], v[136:137] op_sel_hi:[1,0]\n"
    "v_pk_mul_f32 v[62:63], v[62:63], v[110:111] op_sel_hi:[1,0]\n"
    "v_pk_mul_f32 v[132:133], v[132:133], v[110:111] op_sel_hi:[1,0]\n"
    "v_pk_mul_f32 v[134:135], v[134:135], v[110:111] op_sel_hi:[1,0]\n"
    "ds_read_b128 v[136:139], v37 offset:61440\n"
    "s_setprio 1\n"
    "v_cvt_pk_bf16_f32 v100, v100, v101\n"
    "v_cvt_pk_bf16_f32 v101, v102, v103\n"
    "v_cvt_pk_bf16_f32 v102, v88, v89\n"
    "v_mov_b32 v103, 0\n"
    "v_cvt_pk_bf16_f32 v116, v152, v153\n"
    "v_cvt_pk_bf16_f32 v117, v154, v155\n"
    "v_cvt_pk_bf16_f32 v118, v48, v49\n"
    "v_mov_b32 v119, 0\n"
    "ds_read_b128 v[164:167], v38 offset:1408\n"
    "s_mov_b64 exec, s[54:55]\n"
    "ds_read_b128 v[164:167], v38 offset:320\n"
    "s_mov_b64 exec, -1\n"
    "v_cvt_pk_bf16_f32 v160, v160, v161\n"
    "v_cvt_pk_bf16_f32 v161, v162, v163\n"
    "v_cvt_pk_bf16_f32 v162, v60, v61\n"
    "v_mov_b32 v163, 0\n"
    "s_waitcnt lgkmcnt(3)\n"
    "v_mfma_f32_16x16x32_bf16 v[72:75], v[100:103], v[96:99], v[72:75]\n"
    "ds_read_b32 v60, v37 offset:55308\n"
    "ds_read_b128 v[46:49], v38 offset:0\n"
    "v_cvt_pk_bf16_f32 v121, v90, v91\n"
    "v_cvt_pk_bf16_f32 v122, v56, v57\n"
    "v_cvt_pk_bf16_f32 v123, v58, v59\n"
    "v_mov_b32 v120, 0\n"
    "v_mfma_f32_16x16x32_bf16 v[112:115], v[116:119], v[96:99], v[112:115]\n"
    "s_waitcnt lgkmcnt(4)\n"
    "ds_read_b128 v[88:91], v38 offset:64\n"
    "v_cvt_pk_bf16_f32 v153, v50, v51\n"
    "v_cvt_pk_bf16_f32 v154, v124, v125\n"
    "v_cvt_pk_bf16_f32 v155, v126, v127\n"
    "v_mov_b32 v152, 0\n"
    "v_mfma_f32_16x16x32_bf16 v[136:139], v[160:163], v[96:99], v[136:139]\n"
    "v_cvt_pk_bf16_f32 v57, v62, v63\n"
    "v_cvt_pk_bf16_f32 v58, v132, v133\n"
    "v_cvt_pk_bf16_f32 v59, v134, v135\n"
    "v_mov_b32 v56, 0\n"
    "s_waitcnt lgkmcnt(1)\n"
    "v_mfma_f32_16x16x32_bf16 v[60:63], v[46:49], v[96:99], v[60:63]\n"
    "ds_read_b32 v110, v37 offset:55296\n"
    "v_bitop3_b32 v130, v52, v54, v78 bitop3:0xca\n"
    "v_bitop3_b32 v78, v52, v78, v54 bitop3:0xca\n"
    "v_mfma_f32_16x16x32_bf16 v[72:75], v[120:123], v[164:167], v[72:75]\n"
    "ds_read_b128 v[96:99], v37 offset:20480\n"
    "v_mfma_f32_16x16x32_bf16 v[112:115], v[152:155], v[164:167], v[112:115]\n"
    "ds_read_b128 v[52:55], v37 offset:19456\n"
    "v_mfma_f32_16x16x32_bf16 v[136:139], v[56:59], v[164:167], v[136:139]\n"
    "s_waitcnt lgkmcnt(3)\n"
    "ds_read_b128 v[124:127], v37 offset:18432\n"
    "v_mfma_f32_16x16x32_bf16 v[60:63], v[88:91], v[164:167], v[60:63]\n"
    "s_cbranch_vccnz .Lx0None3_%=\n"
    ".Lr0None3_%=:\n"
    "s_setprio 0\n"
    "v_exp_f32 v72, v72\n"
    "v_exp_f32 v73, v73\n"
    "v_exp_f32 v74, v74\n"
    "v_exp_f32 v75, v75\n"
    "v_exp_f32 v112, v112\n"
    "v_exp_f32 v113, v113\n"
    "v_exp_f32 v114, v114\n"
    "v_exp_f32 v115, v115\n"
    "v_cvt_pk_bf16_f32 v72, v72, v73\n"
    "v_cvt_pk_bf16_f32 v73, v74, v75\n"
    "v_cvt_pk_bf16_f32 v74, v112, v113\n"
    "v_cvt_pk_bf16_f32 v75, v114, v115\n"
    "v_exp_f32 v136, v136\n"
    "s_setprio 2\n"
    "s_waitcnt lgkmcnt(0)\n"
    "v_mfma_f32_16x16x32_bf16 v[124:127], v[100:103], v[82:85], v[124:127]\n"
    "v_exp_f32 v137, v137\n"
    "v_mfma_f32_16x16x32_bf16 v[52:55], v[116:119], v[82:85], v[52:55]\n"
    "v_exp_f32 v138, v138\n"
    "v_mfma_f32_16x16x32_bf16 v[96:99], v[160:163], v[82:85], v[96:99]\n"
    "v_exp_f32 v139, v139\n"
    "v_mfma_f32_16x16x32_bf16 v[110:113], v[46:49], v[82:85], v[110:113]\n"
    "v_exp_f32 v60, v60\n"
    "v_cvt_pk_bf16_f32 v144, v136, v137\n"
    "v_cvt_pk_bf16_f32 v145, v138, v139\n"
    "v_cvt_pk_bf16_f32 v146, v60, 0\n"
    "v_mfma_f32_16x16x32_bf16 v[60:63], v[92:95], v[72:75], 0\n"
    "s_cbranch_vccnz .Lx000_%=\n"
    ".Lr000_%=:\n"
    "v_exp_f32 v124, v124\n"
    "v_mfma_f32_16x16x32_bf16 v[60:63], v[128:131], v[144:147], v[60:63]\n"
    "v_exp_f32 v125, v125\n"
    "v_mfma_f32_16x16x32_bf16 v[136:139], v[148:151], v[72:75], 0\n"
    "v_exp_f32 v126, v126\n"
    "v_mfma_f32_16x16x32_bf16 v[136:139], v[156:159], v[144:147], v[136:139]\n"
    "ds_read_b128 v[80:83], v37 offset:21504\n"
    "v_exp_f32 v127, v127\n"
    "v_mfma_f32_16x16x32_bf16 v[112:115], v[140:143], v[72:75], 0\n"
    "ds_read_b128 v[88:91], v37 offset:22528\n"
    "v_exp_f32 v52, v52\n"
    "v_mfma_f32_16x16x32_bf16 v[112:115], v[76:79], v[144:147], v[112:115]\n"
    "ds_read_b128 v[164:167], v37 offset:23552\n"
    "v_exp_f32 v53, v53\n"
    "v_mfma_f32_16x16x32_bf16 v[132:135], v[64:67], v[72:75], 0\n"
    "ds_read_b32 v50, v37 offset:55300\n"
    "v_exp_f32 v54, v54\n"
    "v_mfma_f32_16x16x32_bf16 v[132:135], v[68:71], v[144:147], v[132:135]\n"
    "s_waitcnt lgkmcnt(3)\n"
    "s_setprio 0\n"
    "v_exp_f32 v55, v55\n"
    "v_cvt_pk_bf16_f32 v72, v124, v125\n"
    "v_cvt_pk_bf16_f32 v73, v126, v127\n"
    "v_cvt_pk_bf16_f32 v74, v52, v53\n"
    "v_cvt_pk_bf16_f32 v75, v54, v55\n"
    "v_exp_f32 v96, v96\n"
    "s_setprio 2\n"
    "v_mfma_f32_16x16x32_bf16 v[80:83], v[100:103], v[104:107], v[80:83]\n"
    "s_waitcnt lgkmcnt(2)\n"
    "v_exp_f32 v97, v97\n"
    "v_mfma_f32_16x16x32_bf16 v[88:91], v[116:119], v[104:107], v[88:91]\n"
    "s_waitcnt lgkmcnt(1)\n"
    "v_exp_f32 v98, v98\n"
    "v_mfma_f32_16x16x32_bf16 v[164:167], v[160:163], v[104:107], v[164:167]\n"
    "s_waitcnt lgkmcnt(0)\n"
    "v_exp_f32 v99, v99\n"
    "v_mfma_f32_16x16x32_bf16 v[50:53], v[46:49], v[104:107], v[50:53]\n"
    "v_exp_f32 v110, v110\n"
    "s_mov_b64 exec, s[52:53]\n"
    "ds_write_b128 v38, v[136:139] offset:512\n"
    "ds_write_b128 v38, v[60:63] offset:576\n"
    "s_mov_b64 exec, s[54:55]\n"
    "ds_write_b128 v38, v[132:135] offset:640\n"
    "ds_write_b128 v38, v[112:115] offset:704\n"
    "s_mov_b64 exec, -1\n"
    "v_cvt_pk_bf16_f32 v144, v96, v97\n"
    "v_cvt_pk_bf16_f32 v145, v98, v99\n"
    "v_cvt_pk_bf16_f32 v146, v110, 0\n"
    "v_mfma_f32_16x16x32_bf16 v[96:99], v[92:95], v[72:75], 0\n"
    "s_cbranch_vccnz .Lx001_%=\n"
    ".Lr001_%=:\n"
    "v_exp_f32 v80, v80\n"
    "v_mfma_f32_16x16x32_bf16 v[96:99], v[128:131], v[144:147], v[96:99]\n"
    "ds_read_b128 v[60:63], v37 offset:26624\n"
    "v_exp_f32 v81, v81\n"
    "v_mfma_f32_16x16x32_bf16 v[136:139], v[148:151], v[72:75], 0\n"
    "ds_read_b128 v[72:75], v37 offset:25600\n"
    "v_exp_f32 v82, v82\n"
    "v_mfma_f32_16x16x32_bf16 v[136:139], v[156:159], v[144:147], v[136:139]\n"
    "s_setprio 0\n"
    "ds_read_b128 v[124:127], v37 offset:24576\n"
    "ds_read_b32 v110, v37 offset:55304\n"
    "v_exp_f32 v83, v83\n"
    "v_exp_f32 v88, v88\n"
    "v_exp_f32 v89, v89\n"
    "v_exp_f32 v90, v90\n"
    "v_exp_f32 v91, v91\n"
    "v_cvt_pk_bf16_f32 v52, v80, v81\n"
    "v_cvt_pk_bf16_f32 v53, v82, v83\n"
    "v_cvt_pk_bf16_f32 v54, v88, v89\n"
    "v_cvt_pk_bf16_f32 v55, v90, v91\n"
    "v_rcp_f32 v98, v98\n"
    "v_exp_f32 v164, v164\n"
    "v_exp_f32 v165, v165\n"
    "v_exp_f32 v166, v166\n"
    "v_exp_f32 v167, v167\n"
    "v_exp_f32 v50, v50\n"
    "v_mul_f32 v96, v96, v98\n"
    "s_setprio 2\n"
    "s_waitcnt lgkmcnt(0)\n"
    "v_mfma_f32_16x16x32_bf16 v[124:127], v[100:103], v[40:43], v[124:127]\n"
    "v_mul_f32 v97, v97, v98\n"
    "v_mul_f32 v136, v136, v98\n"
    "v_mfma_f32_16x16x32_bf16 v[72:75], v[116:119], v[40:43], v[72:75]\n"
    "v_mul_f32 v137, v137, v98\n"
    "v_mul_f32 v138, v138, v98\n"
    "v_mfma_f32_16x16x32_bf16 v[60:63], v[160:163], v[40:43], v[60:63]\n"
    "v_mul_f32 v139, v139, v98\n"
    "v_cvt_pk_bf16_f32 v84, v96, v97\n"
    "v_mfma_f32_16x16x32_bf16 v[110:113], v[46:49], v[40:43], v[110:113]\n"
    "v_cvt_pk_bf16_f32 v82, v136, v137\n"
    "v_cvt_pk_bf16_f32 v83, v138, v139\n"
    "v_cvt_pk_bf16_f32 v144, v164, v165\n"
    "v_cvt_pk_bf16_f32 v145, v166, v167\n"
    "v_cvt_pk_bf16_f32 v146, v50, 0\n"
    "v_mfma_f32_16x16x32_bf16 v[116:119], v[92:95], v[52:55], 0\n"
    "s_cbranch_vccnz .Lx002_%=\n"
    ".Lr002_%=:\n"
    "v_exp_f32 v124, v124\n"
    "v_mfma_f32_16x16x32_bf16 v[116:119], v[128:131], v[144:147], v[116:119]\n"
    "ds_read_b128 v[160:163], v37 offset:29696\n"
    "v_exp_f32 v125, v125\n"
    "v_mfma_f32_16x16x32_bf16 v[164:167], v[148:151], v[52:55], 0\n"
    "ds_read_b128 v[96:99], v37 offset:28672\n"
    "ds_read_b128 v[132:135], v38 offset:64\n"
    "v_exp_f32 v126, v126\n"
    "v_mfma_f32_16x16x32_bf16 v[164:167], v[156:159], v[144:147], v[164:167]\n"
    "s_setprio 0\n"
    "ds_read_b128 v[52:55], v37 offset:27648\n"
    "ds_read_b32 v100, v37 offset:56320\n"
    "v_exp_f32 v127, v127\n"
    "v_exp_f32 v72, v72\n"
    "v_exp_f32 v73, v73\n"
    "v_exp_f32 v74, v74\n"
    "v_exp_f32 v75, v75\n"
    "v_cvt_pk_bf16_f32 v88, v124, v125\n"
    "v_cvt_pk_bf16_f32 v89, v126, v127\n"
    "v_cvt_pk_bf16_f32 v90, v72, v73\n"
    "v_cvt_pk_bf16_f32 v91, v74, v75\n"
    "v_rcp_f32 v118, v118\n"
    "v_exp_f32 v60, v60\n"
    "v_exp_f32 v61, v61\n"
    "v_exp_f32 v62, v62\n"
    "v_exp_f32 v63, v63\n"
    "v_exp_f32 v110, v110\n"
    "v_mul_f32 v116, v116, v118\n"
    "s_setprio 2\n"
    "s_waitcnt lgkmcnt(0)\n"
    "v_mfma_f32_16x16x32_bf16 v[52:55], v[120:123], v[84:87], v[52:55]\n"
    "v_mul_f32 v117, v117, v118\n"
    "v_mul_f32 v164, v164, v118\n"
    "v_mfma_f32_16x16x32_bf16 v[96:99], v[152:155], v[84:87], v[96:99]\n"
    "v_mul_f32 v165, v165, v118\n"
    "v_mul_f32 v166, v166, v118\n"
    "v_mfma_f32_16x16x32_bf16 v[160:163], v[56:59], v[84:87], v[160:163]\n"
    "v_mul_f32 v167, v167, v118\n"
    "v_cvt_pk_bf16_f32 v106, v116, v117\n"
    "v_mfma_f32_16x16x32_bf16 v[100:103], v[132:135], v[84:87], v[100:103]\n"
    "v_cvt_pk_bf16_f32 v104, v164, v165\n"
    "v_cvt_pk_bf16_f32 v105, v166, v167\n"
    "v_cvt_pk_bf16_f32 v144, v60, v61\n"
    "v_cvt_pk_bf16_f32 v145, v62, v63\n"
    "v_cvt_pk_bf16_f32 v146, v110, 0\n"
    "s_cmp_eq_u32 s31, 0\n"
    "s_cbranch_scc0 .Lmb_%=\n"
    "buffer_load_dwordx4 v[60:63], v37, s[20:23], 0 offen offset:0 nt\n"
    "buffer_load_dwordx4 v[136:139], v37, s[20:23], 0 offen offset:1024 nt\n"
    "buffer_load_dwordx4 v[72:75], v37, s[20:23], 0 offen offset:2048 nt\n"
    "buffer_load_dwordx4 v[124:127], v37, s[20:23], 0 offen offset:3072 nt\n"
    "v_min_u32 v164, 0xe2b2, v37\n"
    "buffer_load_dwordx4 v[164:167], v164, s[20:23], s37 offen nt\n"
    ".Lmb_%=:\n"
    "v_mfma_f32_16x16x32_bf16 v[112:115], v[92:95], v[88:91], 0\n"
    "s_cbranch_vccnz .Lx010_%=\n"
    ".Lr010_%=:\n"
    "v_exp_f32 v52, v52\n"
    "v_mfma_f32_16x16x32_bf16 v[112:115], v[128:131], v[144:147], v[112:115]\n"
    "ds_read_b128 v[128:131], v37 offset:32768\n"
    "v_exp_f32 v53, v53\n"
    "v_mfma_f32_16x16x32_bf16 v[92:95], v[148:151], v[88:91], 0\n"
    "ds_read_b128 v[116:119], v37 offset:31744\n"
    "v_exp_f32 v54, v54\n"
    "v_mfma_f32_16x16x32_bf16 v[92:95], v[156:159], v[144:147], v[92:95]\n"
    "s_setprio 0\n"
    "ds_read_b128 v[156:159], v37 offset:30720\n"
    "ds_read_b32 v46, v37 offset:56324\n"
    "v_exp_f32 v55, v55\n"
    "v_exp_f32 v96, v96\n"
    "v_exp_f32 v97, v97\n"
    "v_exp_f32 v98, v98\n"
    "v_exp_f32 v99, v99\n"
    "v_cvt_pk_bf16_f32 v148, v52, v53\n"
    "v_cvt_pk_bf16_f32 v149, v54, v55\n"
    "v_cvt_pk_bf16_f32 v150, v96, v97\n"
    "v_cvt_pk_bf16_f32 v151, v98, v99\n"
    "v_rcp_f32 v114, v114\n"
    "v_exp_f32 v160, v160\n"
    "v_exp_f32 v161, v161\n"
    "v_exp_f32 v162, v162\n"
    "v_exp_f32 v163, v163\n"
    "v_exp_f32 v100, v100\n"
    "v_mul_f32 v112, v112, v114\n"
    "s_setprio 2\n"
    "s_waitcnt lgkmcnt(0)\n"
    "v_mfma_f32_16x16x32_bf16 v[156:159], v[120:123], v[106:109], v[156:159]\n"
    "ds_read_b128 v[86:89], v36 offset:9216\n"
    "ds_read_b128 v[96:99], v36 offset:10240\n"
    "v_mul_f32 v113, v113, v114\n"
    "v_mul_f32 v92, v92, v114\n"
    "v_mfma_f32_16x16x32_bf16 v[116:119], v[152:155], v[106:109], v[116:119]\n"
    "ds_read_b128 v[52:55], v36 offset:11264\n"
    "v_mul_f32 v93, v93, v114\n"
    "v_mul_f32 v94, v94, v114\n"
    "v_mfma_f32_16x16x32_bf16 v[128:131], v[56:59], v[106:109], v[128:131]\n"
    "v_mul_f32 v95, v95, v114\n"
    "v_cvt_pk_bf16_f32 v42, v112, v113\n"
    "v_mfma_f32_16x16x32_bf16 v[46:49], v[132:135], v[106:109], v[46:49]\n"
    "v_cvt_pk_bf16_f32 v40, v92, v93\n"
    "v_cvt_pk_bf16_f32 v41, v94, v95\n"
    "v_cvt_pk_bf16_f32 v144, v160, v161\n"
    "v_cvt_pk_bf16_f32 v145, v162, v163\n"
    "v_cvt_pk_bf16_f32 v146, v100, 0\n"
    "v_mfma_f32_16x16x32_bf16 v[90:93], v[140:143], v[148:151], 0\n"
    "s_cbranch_vccnz .Lx011_%=\n"
    ".Lr011_%=:\n"
    "v_exp_f32 v156, v156\n"
    "v_mfma_f32_16x16x32_bf16 v[90:93], v[76:79], v[144:147], v[90:93]\n"
    "v_exp_f32 v157, v157\n"
    "v_mfma_f32_16x16x32_bf16 v[108:111], v[64:67], v[148:151], 0\n"
    "v_exp_f32 v158, v158\n"
    "v_mfma_f32_16x16x32_bf16 v[108:111], v[68:71], v[144:147], v[108:111]\n"
    "s_waitcnt lgkmcnt(0)\n"
    "v_exp_f32 v159, v159\n"
    "v_exp_f32 v116, v116\n"
    "v_exp_f32 v117, v117\n"
    "v_exp_f32 v118, v118\n"
    "v_mfma_f32_16x16x32_bf16 v[112:115], v[86:89], v[0:3], 0\n"
    "v_exp_f32 v119, v119\n"
    "v_mfma_f32_16x16x32_bf16 v[112:115], v[96:99], v[4:7], v[112:115]\n"
    "v_cvt_pk_bf16_f32 v148, v156, v157\n"
    "v_cvt_pk_bf16_f32 v149, v158, v159\n"
    "v_mfma_f32_16x16x32_bf16 v[112:115], v[52:55], v[8:11], v[112:115]\n"
    "ds_read_b128 v[156:159], v37 offset:33792\n"
    "v_cvt_pk_bf16_f32 v150, v116, v117\n"
    "v_cvt_pk_bf16_f32 v151, v118, v119\n"
    "v_mfma_f32_16x16x32_bf16 v[160:163], v[86:89], v[12:15], 0\n"
    "ds_read_b128 v[48:51], v37 offset:34816\n"
    "v_rcp_f32 v90, v90\n"
    "v_mfma_f32_16x16x32_bf16 v[160:163], v[96:99], v[16:19], v[160:163]\n"
    "ds_read_b128 v[116:119], v37 offset:35840\n"
    "v_exp_f32 v128, v128\n"
    "v_mfma_f32_16x16x32_bf16 v[160:163], v[52:55], v[20:23], v[160:163]\n"
    "ds_read_b32 v94, v37 offset:56328\n"
    "v_exp_f32 v129, v129\n"
    "v_mfma_f32_16x16x32_bf16 v[100:103], v[86:89], v[24:27], 0\n"
    "v_exp_f32 v130, v130\n"
    "v_mfma_f32_16x16x32_bf16 v[100:103], v[96:99], v[28:31], v[100:103]\n"
    "v_exp_f32 v131, v131\n"
    "v_mfma_f32_16x16x32_bf16 v[100:103], v[52:55], v[32:35], v[100:103]\n"
    "s_waitcnt lgkmcnt(3)\n"
    "v_exp_f32 v46, v46\n"
    "v_mfma_f32_16x16x32_bf16 v[156:159], v[120:123], v[42:45], v[156:159]\n"
    "s_waitcnt lgkmcnt(2)\n"
    "v_mul_f32 v92, v92, v90\n"
    "v_mul_f32 v93, v93, v90\n"
    "v_mfma_f32_16x16x32_bf16 v[48:51], v[152:155], v[42:45], v[48:51]\n"
    "s_waitcnt lgkmcnt(1)\n"
    "ds_read_b128 v[152:155], v36 offset:12288\n"
    "v_mul_f32 v108, v108, v90\n"
    "v_mul_f32 v109, v109, v90\n"
    "v_mfma_f32_16x16x32_bf16 v[116:119], v[56:59], v[42:45], v[116:119]\n"
    "s_waitcnt lgkmcnt(1)\n"
    "ds_read_b128 v[52:55], v36 offset:13312\n"
    "v_mul_f32 v110, v110, v90\n"
    "v_mul_f32 v111, v111, v90\n"
    "v_mfma_f32_16x16x32_bf16 v[94:97], v[132:135], v[42:45], v[94:97]\n"
    "ds_read_b128 v[132:135], v36 offset:14336\n"
    "v_cvt_pk_bf16_f32 v85, v92, v93\n"
    "v_cvt_pk_bf16_f32 v86, v108, v109\n"
    "v_cvt_pk_bf16_f32 v87, v110, v111\n"
    "v_cvt_pk_bf16_f32 v144, v128, v129\n"
    "v_cvt_pk_bf16_f32 v145, v130, v131\n"
    "v_cvt_pk_bf16_f32 v146, v46, 0\n"
    "v_mfma_f32_16x16x32_bf16 v[44:47], v[140:143], v[148:151], 0\n"
    "s_cbranch_vccnz .Lx012_%=\n"
    ".Lr012_%=:\n"
    "v_exp_f32 v156, v156\n"
    "v_mfma_f32_16x16x32_bf16 v[44:47], v[76:79], v[144:147], v[44:47]\n"
    "v_exp_f32 v157, v157\n"
    "v_mfma_f32_16x16x32_bf16 v[108:111], v[64:67], v[148:151], 0\n"
    "v_exp_f32 v158, v158\n"
    "v_mfma_f32_16x16x32_bf16 v[108:111], v[68:71], v[144:147], v[108:111]\n"
    "s_setprio 0\n"
    "v_exp_f32 v159, v159\n"
    "v_exp_f32 v48, v48\n"
    "v_exp_f32 v49, v49\n"
    "v_exp_f32 v50, v50\n"
    "v_exp_f32 v51, v51\n"
    "v_cvt_pk_bf16_f32 v96, v156, v157\n"
    "v_cvt_pk_bf16_f32 v97, v158, v159\n"
    "v_cvt_pk_bf16_f32 v98, v48, v49\n"
    "v_cvt_pk_bf16_f32 v99, v50, v51\n"
    "v_rcp_f32 v44, v44\n"
    "s_setprio 2\n"
    "s_waitcnt lgkmcnt(0)\n"
    "v_mfma_f32_16x16x32_bf16 v[90:93], v[152:155], v[0:3], 0\n"
    "v_exp_f32 v116, v116\n"
    "v_mfma_f32_16x16x32_bf16 v[90:93], v[52:55], v[4:7], v[90:93]\n"
    "v_exp_f32 v117, v117\n"
    "v_mfma_f32_16x16x32_bf16 v[90:93], v[132:135], v[8:11], v[90:93]\n"
    "ds_read_b128 v[128:131], v36 offset:15360\n"
    "v_exp_f32 v118, v118\n"
    "v_mfma_f32_16x16x32_bf16 v[56:59], v[152:155], v[12:15], 0\n"
    "ds_read_b128 v[156:159], v36 offset:16384\n"
    "v_exp_f32 v119, v119\n"
    "v_mfma_f32_16x16x32_bf16 v[56:59], v[52:55], v[16:19], v[56:59]\n"
    "ds_read_b128 v[48:51], v36 offset:17408\n"
    "v_exp_f32 v94, v94\n"
    "v_mfma_f32_16x16x32_bf16 v[56:59], v[132:135], v[20:23], v[56:59]\n"
    "v_mul_f32 v46, v46, v44\n"
    "v_mul_f32 v47, v47, v44\n"
    "v_mfma_f32_16x16x32_bf16 v[120:123], v[152:155], v[24:27], 0\n"
    "ds_read_b128 v[148:151], v36 offset:56320\n"
    "v_mul_f32 v108, v108, v44\n"
    "v_mul_f32 v109, v109, v44\n"
    "v_mfma_f32_16x16x32_bf16 v[120:123], v[52:55], v[28:31], v[120:123]\n"
    "v_mul_f32 v110, v110, v44\n"
    "v_mul_f32 v111, v111, v44\n"
    "v_mfma_f32_16x16x32_bf16 v[120:123], v[132:135], v[32:35], v[120:123]\n"
    "v_cvt_pk_bf16_f32 v107, v46, v47\n"
    "v_cvt_pk_bf16_f32 v108, v108, v109\n"
    "v_cvt_pk_bf16_f32 v109, v110, v111\n"
    "v_cvt_pk_bf16_f32 v144, v116, v117\n"
    "v_cvt_pk_bf16_f32 v145, v118, v119\n"
    "v_cvt_pk_bf16_f32 v146, v94, 0\n"
    "s_setprio 1\n"
    "v_mfma_f32_16x16x32_bf16 v[132:135], v[140:143], v[96:99], 0\n"
    "v_fmaak_f32 v95, v112, v112, 0x2a8cbccc\n"
    "v_fmac_f32 v95, v113, v113\n"
    "v_mfma_f32_16x16x32_bf16 v[132:135], v[76:79], v[144:147], v[132:135]\n"
    "v_fmac_f32 v95, v114, v114\n"
    "v_fmac_f32 v95, v115, v115\n"
    "v_mfma_f32_16x16x32_bf16 v[44:47], v[64:67], v[96:99], 0\n"
    "v_fmaak_f32 v110, v160, v160, 0x2a8cbccc\n"
    "v_fmac_f32 v110, v161, v161\n"
    "v_mfma_f32_16x16x32_bf16 v[44:47], v[68:71], v[144:147], v[44:47]\n"
    "s_waitcnt lgkmcnt(0)\n"
    "v_fmac_f32 v110, v162, v162\n"
    "v_fmac_f32 v110, v163, v163\n"
    "v_mfma_f32_16x16x32_bf16 v[116:119], v[128:131], v[0:3], 0\n"
    "ds_read_b128 v[64:67], v36 offset:49152\n"
    "ds_read_b128 v[144:147], v36 offset:50176\n"
    "v_add_u32 v39, v38, v149\n"
    "v_mfma_f32_16x16x32_bf16 v[116:119], v[156:159], v[4:7], v[116:119]\n"
    "v_rcp_f32 v132, v132\n"
    "v_mfma_f32_16x16x32_bf16 v[116:119], v[48:51], v[8:11], v[116:119]\n"
    "ds_read_b128 v[76:79], v36 offset:51200\n"
    "v_mul_f32 v134, v134, v132\n"
    "v_mul_f32 v135, v135, v132\n"
    "v_mfma_f32_16x16x32_bf16 v[152:155], v[128:131], v[12:15], 0\n"
    "v_mul_f32 v44, v44, v132\n"
    "v_mul_f32 v45, v45, v132\n"
    "v_mfma_f32_16x16x32_bf16 v[152:155], v[156:159], v[16:19], v[152:155]\n"
    "v_mul_f32 v46, v46, v132\n"
    "v_mul_f32 v47, v47, v132\n"
    "v_mfma_f32_16x16x32_bf16 v[152:155], v[48:51], v[20:23], v[152:155]\n"
    "v_cvt_pk_bf16_f32 v43, v134, v135\n"
    "v_cvt_pk_bf16_f32 v44, v44, v45\n"
    "v_mfma_f32_16x16x32_bf16 v[140:143], v[128:131], v[24:27], 0\n"
    "v_cvt_pk_bf16_f32 v45, v46, v47\n"
    "v_fmaak_f32 v71, v100, v100, 0x2a8cbccc\n"
    "v_mfma_f32_16x16x32_bf16 v[140:143], v[156:159], v[28:31], v[140:143]\n"
    "v_fmac_f32 v71, v101, v101\n"
    "v_fmac_f32 v71, v102, v102\n"
    "v_mfma_f32_16x16x32_bf16 v[140:143], v[48:51], v[32:35], v[140:143]\n"
    "v_fmac_f32 v71, v103, v103\n"
    "v_fmac_f32 v95, v90, v90\n"
    "v_fmac_f32 v95, v91, v91\n"
    "v_fmaak_f32 v52, v92, v92, 0x2a8cbccc\n"
    "v_fmac_f32 v52, v93, v93\n"
    "v_fmac_f32 v110, v56, v56\n"
    "v_fmac_f32 v110, v57, v57\n"
    "v_fmaak_f32 v111, v58, v58, 0x2a8cbccc\n"
    "v_fmac_f32 v111, v59, v59\n"
    "v_fmac_f32 v71, v120, v120\n"
    "v_fmac_f32 v71, v121, v121\n"
    "v_fmaak_f32 v54, v122, v122, 0x2a8cbccc\n"
    "v_fmac_f32 v54, v123, v123\n"
    "s_cmp_eq_u32 s31, 0\n"
    "s_cbranch_scc0 .Lfb_%=\n"
    "s_waitcnt vmcnt(0)\n"
    "v_bitop3_b32 v60, v60, v61, v62 bitop3:0xfe\n"
    "v_bitop3_b32 v60, v60, v63, v136 bitop3:0xfe\n"
    "v_bitop3_b32 v60, v60, v137, v138 bitop3:0xfe\n"
    "v_bitop3_b32 v60, v60, v139, v72 bitop3:0xfe\n"
    "v_bitop3_b32 v60, v60, v73, v74 bitop3:0xfe\n"
    "v_bitop3_b32 v60, v60, v75, v124 bitop3:0xfe\n"
    "v_bitop3_b32 v60, v60, v125, v126 bitop3:0xfe\n"
    "v_bitop3_b32 v60, v60, v127, v164 bitop3:0xfe\n"
    "v_bitop3_b32 v60, v60, v165, v166 bitop3:0xfe\n"
    "v_or_b32 v60, v60, v167\n"
    "v_and_b32 v60, 0x7fff7fff, v60\n"
    "v_cmp_ne_u32 s[48:49], 0, v60\n"
    "s_cmp_lg_u64 s[48:49], 0\n"
    "s_cselect_b32 s35, 1, s35\n"
    ".Lfb_%=:\n"
    "s_waitcnt lgkmcnt(0)\n"
    "v_mfma_f32_16x16x32_bf16 v[156:159], v[0:3], v[64:67], 0\n"
    "ds_read_b64 v[96:97], v36 offset:55296\n"
    "ds_read_b128 v[164:167], v36 offset:46080\n"
    "v_mfma_f32_16x16x32_bf16 v[156:159], v[4:7], v[144:147], v[156:159]\n"
    "ds_read_b128 v[124:127], v36 offset:47104\n"
    "ds_read_b128 v[72:75], v36 offset:48128\n"
    "v_fmac_f32 v52, v116, v116\n"
    "v_fmac_f32 v52, v117, v117\n"
    "v_mfma_f32_16x16x32_bf16 v[156:159], v[8:11], v[76:79], v[156:159]\n"
    "v_fmac_f32 v52, v118, v118\n"
    "v_fmac_f32 v52, v119, v119\n"
    "v_mfma_f32_16x16x32_bf16 v[136:139], v[12:15], v[64:67], 0\n"
    "v_fmac_f32 v111, v152, v152\n"
    "v_fmac_f32 v111, v153, v153\n"
    "v_mfma_f32_16x16x32_bf16 v[136:139], v[16:19], v[144:147], v[136:139]\n"
    "v_fmac_f32 v111, v154, v154\n"
    "v_fmac_f32 v111, v155, v155\n"
    "v_mfma_f32_16x16x32_bf16 v[136:139], v[20:23], v[76:79], v[136:139]\n"
    "v_fmac_f32 v54, v140, v140\n"
    "v_fmac_f32 v54, v141, v141\n"
    "v_mfma_f32_16x16x32_bf16 v[132:135], v[24:27], v[64:67], 0\n"
    "v_fmac_f32 v54, v142, v142\n"
    "v_fmac_f32 v54, v143, v143\n"
    "v_mfma_f32_16x16x32_bf16 v[132:135], v[28:31], v[144:147], v[132:135]\n"
    "v_cvt_pk_bf16_f32 v52, v95, v52\n"
    "v_cvt_pk_bf16_f32 v53, v110, v111\n"
    "v_mfma_f32_16x16x32_bf16 v[132:135], v[32:35], v[76:79], v[132:135]\n"
    "s_waitcnt lgkmcnt(0)\n"
    "ds_read_b128 v[60:63], v36 offset:52224\n"
    "v_cvt_pk_bf16_f32 v98, v71, v54\n"
    "v_cvt_pk_bf16_f32 v99, v71, v54\n"
    "v_mfma_f32_16x16x16_bf16 v[128:131], v[96:97], v[52:53], 0\n"
    "ds_read_b128 v[144:147], v36 offset:53248\n"
    "v_cvt_pk_bf16_f32 v52, v156, v157\n"
    "v_cvt_pk_bf16_f32 v53, v158, v159\n"
    "v_mfma_f32_16x16x16_bf16 v[156:159], v[96:97], v[98:99], 0\n"
    "ds_read_b128 v[78:81], v36 offset:54272\n"
    "v_cvt_pk_bf16_f32 v54, v136, v137\n"
    "v_cvt_pk_bf16_f32 v55, v138, v139\n"
    "v_mfma_f32_16x16x32_bf16 v[136:139], v[0:3], v[164:167], 0\n"
    "v_cvt_pk_bf16_f32 v76, v132, v133\n"
    "v_cvt_pk_bf16_f32 v77, v134, v135\n"
    "v_mfma_f32_16x16x32_bf16 v[136:139], v[4:7], v[124:127], v[136:139]\n"
    "v_bitop3_b32 v94, v148, v150, v52 bitop3:0xca\n"
    "v_bitop3_b32 v52, v148, v52, v150 bitop3:0xca\n"
    "v_mfma_f32_16x16x32_bf16 v[136:139], v[8:11], v[72:75], v[136:139]\n"
    "v_bitop3_b32 v95, v148, v150, v53 bitop3:0xca\n"
    "v_bitop3_b32 v53, v148, v53, v150 bitop3:0xca\n"
    "v_mfma_f32_16x16x32_bf16 v[96:99], v[12:15], v[164:167], 0\n"
    "v_rsq_f32 v128, v128\n"
    "v_mfma_f32_16x16x32_bf16 v[96:99], v[16:19], v[124:127], v[96:99]\n"
    "v_rsq_f32 v129, v129\n"
    "v_mfma_f32_16x16x32_bf16 v[96:99], v[20:23], v[72:75], v[96:99]\n"
    "v_rsq_f32 v130, v130\n"
    "v_mfma_f32_16x16x32_bf16 v[68:71], v[24:27], v[164:167], 0\n"
    "v_rsq_f32 v131, v131\n"
    "v_mfma_f32_16x16x32_bf16 v[68:71], v[28:31], v[124:127], v[68:71]\n"
    "v_rsq_f32 v156, v156\n"
    "v_mfma_f32_16x16x32_bf16 v[68:71], v[32:35], v[72:75], v[68:71]\n"
    "s_waitcnt lgkmcnt(0)\n"
    "v_rsq_f32 v157, v157\n"
    "v_mfma_f32_16x16x32_bf16 v[164:167], v[0:3], v[60:63], 0\n"
    "v_mul_f32 v128, s46, v128\n"
    "v_mul_f32 v46, s47, v129\n"
    "v_mfma_f32_16x16x32_bf16 v[164:167], v[4:7], v[144:147], v[164:167]\n"
    "v_mul_f32 v130, s46, v130\n"
    "v_mul_f32 v158, s47, v131\n"
    "v_mfma_f32_16x16x32_bf16 v[164:167], v[8:11], v[78:81], v[164:167]\n"
    "ds_read_b128 v[72:75], v36 offset:27648\n"
    "v_mul_f32 v156, s46, v156\n"
    "v_mul_f32 v110, s47, v157\n"
    "v_mfma_f32_16x16x32_bf16 v[64:67], v[12:15], v[60:63], 0\n"
    "ds_read_b128 v[132:135], v36 offset:28672\n"
    "v_cvt_pk_bf16_f32 v124, v136, v137\n"
    "v_cvt_pk_bf16_f32 v125, v138, v139\n"
    "v_mfma_f32_16x16x32_bf16 v[64:67], v[16:19], v[144:147], v[64:67]\n"
    "ds_read_b128 v[136:139], v36 offset:29696\n"
    "v_cvt_pk_bf16_f32 v126, v96, v97\n"
    "v_cvt_pk_bf16_f32 v127, v98, v99\n"
    "v_mfma_f32_16x16x32_bf16 v[64:67], v[20:23], v[78:81], v[64:67]\n"
    "v_cvt_pk_bf16_f32 v98, v68, v69\n"
    "v_cvt_pk_bf16_f32 v99, v70, v71\n"
    "v_mfma_f32_16x16x32_bf16 v[68:71], v[24:27], v[60:63], 0\n"
    "v_bitop3_b32 v96, v148, v150, v54 bitop3:0xca\n"
    "v_bitop3_b32 v54, v148, v54, v150 bitop3:0xca\n"
    "v_mfma_f32_16x16x32_bf16 v[68:71], v[28:31], v[144:147], v[68:71]\n"
    "v_bitop3_b32 v97, v148, v150, v55 bitop3:0xca\n"
    "v_bitop3_b32 v55, v148, v55, v150 bitop3:0xca\n"
    "v_mfma_f32_16x16x32_bf16 v[68:71], v[32:35], v[78:81], v[68:71]\n"
    "ds_read_b128 v[78:81], v36 offset:31744\n"
    "v_bitop3_b32 v60, v148, v150, v76 bitop3:0xca\n"
    "v_bitop3_b32 v76, v148, v76, v150 bitop3:0xca\n"
    "v_bitop3_b32 v61, v148, v150, v77 bitop3:0xca\n"
    "v_bitop3_b32 v77, v148, v77, v150 bitop3:0xca\n"
    "s_setprio 3\n"
    "v_pk_mul_f32 v[112:113], v[112:113], v[128:129] op_sel_hi:[1,0]\n"
    "v_pk_mul_f32 v[114:115], v[114:115], v[128:129] op_sel_hi:[1,0]\n"
    "v_pk_mul_f32 v[90:91], v[90:91], v[128:129] op_sel_hi:[1,0]\n"
    "v_pk_mul_f32 v[92:93], v[92:93], v[46:47] op_sel_hi:[1,0]\n"
    "v_pk_mul_f32 v[116:117], v[116:117], v[46:47] op_sel_hi:[1,0]\n"
    "v_pk_mul_f32 v[118:119], v[118:119], v[46:47] op_sel_hi:[1,0]\n"
    "v_pk_mul_f32 v[160:161], v[160:161], v[130:131] op_sel_hi:[1,0]\n"
    "v_pk_mul_f32 v[162:163], v[162:163], v[130:131] op_sel_hi:[1,0]\n"
    "v_pk_mul_f32 v[56:57], v[56:57], v[130:131] op_sel_hi:[1,0]\n"
    "v_pk_mul_f32 v[58:59], v[58:59], v[158:159] op_sel_hi:[1,0]\n"
    "v_pk_mul_f32 v[152:153], v[152:153], v[158:159] op_sel_hi:[1,0]\n"
    "v_pk_mul_f32 v[154:155], v[154:155], v[158:159] op_sel_hi:[1,0]\n"
    "v_pk_mul_f32 v[100:101], v[100:101], v[156:157] op_sel_hi:[1,0]\n"
    "v_pk_mul_f32 v[102:103], v[102:103], v[156:157] op_sel_hi:[1,0]\n"
    "v_pk_mul_f32 v[120:121], v[120:121], v[156:157] op_sel_hi:[1,0]\n"
    "v_pk_mul_f32 v[122:123], v[122:123], v[110:111] op_sel_hi:[1,0]\n"
    "v_pk_mul_f32 v[140:141], v[140:141], v[110:111] op_sel_hi:[1,0]\n"
    "v_pk_mul_f32 v[142:143], v[142:143], v[110:111] op_sel_hi:[1,0]\n"
    "s_setprio 1\n"
    "s_waitcnt lgkmcnt(0)\n"
    "v_mfma_f32_16x16x32_bf16 v[128:131], v[72:75], v[0:3], 0\n"
    "ds_read_b128 v[144:147], v36 offset:30720\n"
    "ds_read_b128 v[156:159], v36 offset:32768\n"
    "v_cvt_pk_bf16_f32 v88, v112, v113\n"
    "v_cvt_pk_bf16_f32 v89, v114, v115\n"
    "v_mfma_f32_16x16x32_bf16 v[128:131], v[132:135], v[4:7], v[128:131]\n"
    "v_cvt_pk_bf16_f32 v90, v90, v91\n"
    "v_cvt_pk_bf16_f32 v91, v92, v93\n"
    "v_mfma_f32_16x16x32_bf16 v[128:131], v[136:139], v[8:11], v[128:131]\n"
    "v_cvt_pk_bf16_f32 v92, v116, v117\n"
    "v_cvt_pk_bf16_f32 v93, v118, v119\n"
    "v_mfma_f32_16x16x32_bf16 v[116:119], v[72:75], v[12:15], 0\n"
    "v_cvt_pk_bf16_f32 v110, v160, v161\n"
    "v_cvt_pk_bf16_f32 v111, v162, v163\n"
    "v_mfma_f32_16x16x32_bf16 v[116:119], v[132:135], v[16:19], v[116:119]\n"
    "v_cvt_pk_bf16_f32 v112, v56, v57\n"
    "v_cvt_pk_bf16_f32 v113, v58, v59\n"
    "v_mfma_f32_16x16x32_bf16 v[116:119], v[136:139], v[20:23], v[116:119]\n"
    "v_cvt_pk_bf16_f32 v114, v152, v153\n"
    "v_cvt_pk_bf16_f32 v115, v154, v155\n"
    "v_mfma_f32_16x16x32_bf16 v[160:163], v[72:75], v[24:27], 0\n"
    "ds_read_b128 v[152:155], v36 offset:33792\n"
    "ds_read_b128 v[72:75], v36 offset:34816\n"
    "v_cvt_pk_bf16_f32 v46, v100, v101\n"
    "v_cvt_pk_bf16_f32 v47, v102, v103\n"
    "v_mfma_f32_16x16x32_bf16 v[160:163], v[132:135], v[28:31], v[160:163]\n"
    "v_cvt_pk_bf16_f32 v48, v120, v121\n"
    "v_cvt_pk_bf16_f32 v49, v122, v123\n"
    "v_mfma_f32_16x16x32_bf16 v[160:163], v[136:139], v[32:35], v[160:163]\n"
    "s_waitcnt lgkmcnt(2)\n"
    "ds_read_b128 v[100:103], v36 offset:35840\n"
    "v_cvt_pk_bf16_f32 v50, v140, v141\n"
    "v_cvt_pk_bf16_f32 v51, v142, v143\n"
    "v_mfma_f32_16x16x32_bf16 v[132:135], v[144:147], v[0:3], 0\n"
    "v_cvt_pk_bf16_f32 v136, v164, v165\n"
    "v_cvt_pk_bf16_f32 v137, v166, v167\n"
    "v_mfma_f32_16x16x32_bf16 v[132:135], v[78:81], v[4:7], v[132:135]\n"
    "v_cvt_pk_bf16_f32 v138, v64, v65\n"
    "v_cvt_pk_bf16_f32 v139, v66, v67\n"
    "v_mfma_f32_16x16x32_bf16 v[132:135], v[156:159], v[8:11], v[132:135]\n"
    "v_cvt_pk_bf16_f32 v56, v68, v69\n"
    "v_cvt_pk_bf16_f32 v57, v70, v71\n"
    "v_mfma_f32_16x16x32_bf16 v[64:67], v[144:147], v[12:15], 0\n"
    "v_fmaak_f32 v59, v128, v128, 0x2a8cbccc\n"
    "v_fmac_f32 v59, v129, v129\n"
    "v_mfma_f32_16x16x32_bf16 v[64:67], v[78:81], v[16:19], v[64:67]\n"
    "v_fmac_f32 v59, v130, v130\n"
    "v_fmac_f32 v59, v131, v131\n"
    "v_mfma_f32_16x16x32_bf16 v[64:67], v[156:159], v[20:23], v[64:67]\n"
    "v_fmaak_f32 v120, v116, v116, 0x2a8cbccc\n"
    "v_fmac_f32 v120, v117, v117\n"
    "v_mfma_f32_16x16x32_bf16 v[164:167], v[144:147], v[24:27], 0\n"
    "v_fmac_f32 v120, v118, v118\n"
    "v_fmac_f32 v120, v119, v119\n"
    "v_mfma_f32_16x16x32_bf16 v[164:167], v[78:81], v[28:31], v[164:167]\n"
    "v_fmaak_f32 v62, v160, v160, 0x2a8cbccc\n"
    "v_fmac_f32 v62, v161, v161\n"
    "v_mfma_f32_16x16x32_bf16 v[164:167], v[156:159], v[32:35], v[164:167]\n"
    "s_waitcnt lgkmcnt(0)\n"
    "v_fmac_f32 v62, v162, v162\n"
    "v_fmac_f32 v62, v163, v163\n"
    "v_mfma_f32_16x16x32_bf16 v[68:71], v[152:155], v[0:3], 0\n"
    "v_mfma_f32_16x16x32_bf16 v[68:71], v[72:75], v[4:7], v[68:71]\n"
    "v_fmac_f32 v59, v132, v132\n"
    "v_fmac_f32 v59, v133, v133\n"
    "v_mfma_f32_16x16x32_bf16 v[68:71], v[100:103], v[8:11], v[68:71]\n"
    "v_fmaak_f32 v7, v134, v134, 0x2a8cbccc\n"
    "v_fmac_f32 v7, v135, v135\n"
    "v_mfma_f32_16x16x32_bf16 v[156:159], v[152:155], v[12:15], 0\n"
    "v_fmac_f32 v120, v64, v64\n"
    "v_fmac_f32 v120, v65, v65\n"
    "v_mfma_f32_16x16x32_bf16 v[156:159], v[72:75], v[16:19], v[156:159]\n"
    "ds_read_b64 v[8:9], v36 offset:55296\n"
    "v_fmaak_f32 v12, v66, v66, 0x2a8cbccc\n"
    "v_fmac_f32 v12, v67, v67\n"
    "v_mfma_f32_16x16x32_bf16 v[156:159], v[100:103], v[20:23], v[156:159]\n"
    "v_fmac_f32 v62, v164, v164\n"
    "v_fmac_f32 v62, v165, v165\n"
    "v_mfma_f32_16x16x32_bf16 v[144:147], v[152:155], v[24:27], 0\n"
    "v_fmaak_f32 v5, v166, v166, 0x2a8cbccc\n"
    "v_fmac_f32 v5, v167, v167\n"
    "v_mfma_f32_16x16x32_bf16 v[144:147], v[72:75], v[28:31], v[144:147]\n"
    "v_mfma_f32_16x16x32_bf16 v[144:147], v[100:103], v[32:35], v[144:147]\n"
    "v_mov_b32 v75, 0\n"
    "v_fmac_f32 v7, v68, v68\n"
    "v_fmac_f32 v7, v69, v69\n"
    "v_fmac_f32 v7, v70, v70\n"
    "v_fmac_f32 v7, v71, v71\n"
    "v_fmac_f32 v12, v156, v156\n"
    "v_fmac_f32 v12, v157, v157\n"
    "v_fmac_f32 v12, v158, v158\n"
    "v_fmac_f32 v12, v159, v159\n"
    "v_fmac_f32 v5, v144, v144\n"
    "v_fmac_f32 v5, v145, v145\n"
    "v_fmac_f32 v5, v146, v146\n"
    "v_fmac_f32 v5, v147, v147\n"
    "v_cvt_pk_bf16_f32 v28, v59, v7\n"
    "v_cvt_pk_bf16_f32 v29, v120, v12\n"
    "v_cvt_pk_bf16_f32 v152, v62, v5\n"
    "v_cvt_pk_bf16_f32 v153, v62, v5\n"
    "s_waitcnt lgkmcnt(0)\n"
    "v_mfma_f32_16x16x16_bf16 v[16:19], v[8:9], v[28:29], 0\n"
    "ds_read2_b32 v[78:79], v39 offset0:64 offset1:64\n"
    "ds_read_b32 v63, v39 offset:256\n"
    "ds_read_b128 v[20:23], v37 offset:62464\n"
    "v_mfma_f32_16x16x16_bf16 v[28:31], v[8:9], v[152:153], 0\n"
    "ds_read2_b32 v[100:101], v39 offset0:48 offset1:48\n"
    "ds_read2_b32 v[58:59], v39 offset0:80 offset1:80\n"
    "ds_read_b128 v[32:35], v38 offset:1408\n"
    "s_mov_b64 exec, s[52:53]\n"
    "ds_read_b128 v[32:35], v38 offset:384\n"
    "s_mov_b64 exec, -1\n"
    "v_rsq_f32 v16, v16\n"
    "v_rsq_f32 v10, v17\n"
    "v_rsq_f32 v18, v18\n"
    "v_rsq_f32 v102, v19\n"
    "v_rsq_f32 v28, v28\n"
    "v_rsq_f32 v74, v29\n"
    "ds_read_b128 v[120:123], v37 offset:63488\n"
    "s_setprio 3\n"
    "v_pk_mul_f32 v[128:129], v[128:129], v[16:17] op_sel_hi:[1,0]\n"
    "v_pk_mul_f32 v[130:131], v[130:131], v[16:17] op_sel_hi:[1,0]\n"
    "v_pk_mul_f32 v[132:133], v[132:133], v[16:17] op_sel_hi:[1,0]\n"
    "v_pk_mul_f32 v[134:135], v[134:135], v[10:11] op_sel_hi:[1,0]\n"
    "v_pk_mul_f32 v[68:69], v[68:69], v[10:11] op_sel_hi:[1,0]\n"
    "v_pk_mul_f32 v[70:71], v[70:71], v[10:11] op_sel_hi:[1,0]\n"
    "v_pk_mul_f32 v[116:117], v[116:117], v[18:19] op_sel_hi:[1,0]\n"
    "v_pk_mul_f32 v[118:119], v[118:119], v[18:19] op_sel_hi:[1,0]\n"
    "v_pk_mul_f32 v[64:65], v[64:65], v[18:19] op_sel_hi:[1,0]\n"
    "v_pk_mul_f32 v[66:67], v[66:67], v[102:103] op_sel_hi:[1,0]\n"
    "v_pk_mul_f32 v[156:157], v[156:157], v[102:103] op_sel_hi:[1,0]\n"
    "v_pk_mul_f32 v[158:159], v[158:159], v[102:103] op_sel_hi:[1,0]\n"
    "v_pk_mul_f32 v[160:161], v[160:161], v[28:29] op_sel_hi:[1,0]\n"
    "v_pk_mul_f32 v[162:163], v[162:163], v[28:29] op_sel_hi:[1,0]\n"
    "v_pk_mul_f32 v[164:165], v[164:165], v[28:29] op_sel_hi:[1,0]\n"
    "v_pk_mul_f32 v[166:167], v[166:167], v[74:75] op_sel_hi:[1,0]\n"
    "v_pk_mul_f32 v[144:145], v[144:145], v[74:75] op_sel_hi:[1,0]\n"
    "v_pk_mul_f32 v[146:147], v[146:147], v[74:75] op_sel_hi:[1,0]\n"
    "ds_read_b128 v[0:3], v37 offset:64512\n"
    "s_setprio 1\n"
    "v_cvt_pk_bf16_f32 v4, v128, v129\n"
    "v_cvt_pk_bf16_f32 v5, v130, v131\n"
    "v_cvt_pk_bf16_f32 v6, v132, v133\n"
    "v_mov_b32 v7, 0\n"
    "v_cvt_pk_bf16_f32 v128, v116, v117\n"
    "v_cvt_pk_bf16_f32 v129, v118, v119\n"
    "v_cvt_pk_bf16_f32 v130, v64, v65\n"
    "v_mov_b32 v131, 0\n"
    "v_cvt_pk_bf16_f32 v12, v160, v161\n"
    "ds_read_b128 v[116:119], v38 offset:1408\n"
    "s_mov_b64 exec, s[54:55]\n"
    "ds_read_b128 v[116:119], v38 offset:448\n"
    "s_mov_b64 exec, -1\n"
    "v_cvt_pk_bf16_f32 v13, v162, v163\n"
    "v_cvt_pk_bf16_f32 v14, v164, v165\n"
    "v_mov_b32 v15, 0\n"
    "v_mov_b32 v80, 0x27c40\n"
    "s_waitcnt lgkmcnt(3)\n"
    "v_mfma_f32_16x16x32_bf16 v[20:23], v[4:7], v[32:35], v[20:23]\n"
    "ds_read_b32 v160, v37 offset:57356\n"
    "ds_read_b128 v[16:19], v38 offset:128\n"
    "v_cvt_pk_bf16_f32 v9, v134, v135\n"
    "v_cvt_pk_bf16_f32 v10, v68, v69\n"
    "v_cvt_pk_bf16_f32 v11, v70, v71\n"
    "v_mov_b32 v8, 0\n"
    "v_mfma_f32_16x16x32_bf16 v[120:123], v[128:131], v[32:35], v[120:123]\n"
    "s_waitcnt lgkmcnt(4)\n"
    "ds_read_b128 v[140:143], v38 offset:192\n"
    "v_cvt_pk_bf16_f32 v25, v66, v67\n"
    "v_cvt_pk_bf16_f32 v26, v156, v157\n"
    "v_cvt_pk_bf16_f32 v27, v158, v159\n"
    "v_mov_b32 v24, 0\n"
    "v_mfma_f32_16x16x32_bf16 v[0:3], v[12:15], v[32:35], v[0:3]\n"
    "v_cvt_pk_bf16_f32 v29, v166, v167\n"
    "v_cvt_pk_bf16_f32 v30, v144, v145\n"
    "v_cvt_pk_bf16_f32 v31, v146, v147\n"
    "v_mov_b32 v28, 0\n"
    "s_waitcnt lgkmcnt(1)\n"
    "v_mfma_f32_16x16x32_bf16 v[160:163], v[16:19], v[32:35], v[160:163]\n"
    "ds_read_b32 v32, v37 offset:57344\n"
    "v_bitop3_b32 v62, v148, v150, v78 bitop3:0xca\n"
    "v_bitop3_b32 v78, v148, v78, v150 bitop3:0xca\n"
    "v_mfma_f32_16x16x32_bf16 v[20:23], v[8:11], v[116:119], v[20:23]\n"
    "ds_read_b128 v[64:67], v37 offset:38912\n"
    "v_mfma_f32_16x16x32_bf16 v[120:123], v[24:27], v[116:119], v[120:123]\n"
    "ds_read_b128 v[152:155], v37 offset:37888\n"
    "v_mfma_f32_16x16x32_bf16 v[0:3], v[28:31], v[116:119], v[0:3]\n"
    "s_waitcnt lgkmcnt(3)\n"
    "ds_read_b128 v[164:167], v37 offset:36864\n"
    "v_mfma_f32_16x16x32_bf16 v[160:163], v[140:143], v[116:119], v[160:163]\n"
    "s_cbranch_vccnz .Lx1None3_%=\n"
    ".Lr1None3_%=:\n"
    "s_setprio 0\n"
    "v_exp_f32 v20, v20\n"
    "v_exp_f32 v21, v21\n"
    "v_exp_f32 v22, v22\n"
    "v_exp_f32 v23, v23\n"
    "v_exp_f32 v120, v120\n"
    "v_exp_f32 v121, v121\n"
    "v_exp_f32 v122, v122\n"
    "v_exp_f32 v123, v123\n"
    "v_cvt_pk_bf16_f32 v144, v20, v21\n"
    "v_cvt_pk_bf16_f32 v145, v22, v23\n"
    "v_cvt_pk_bf16_f32 v146, v120, v121\n"
    "v_cvt_pk_bf16_f32 v147, v122, v123\n"
    "v_exp_f32 v0, v0\n"
    "s_setprio 2\n"
    "s_waitcnt lgkmcnt(0)\n"
    "v_mfma_f32_16x16x32_bf16 v[164:167], v[4:7], v[88:91], v[164:167]\n"
    "v_exp_f32 v1, v1\n"
    "v_mfma_f32_16x16x32_bf16 v[152:155], v[128:131], v[88:91], v[152:155]\n"
    "v_exp_f32 v2, v2\n"
    "v_mfma_f32_16x16x32_bf16 v[64:67], v[12:15], v[88:91], v[64:67]\n"
    "v_exp_f32 v3, v3\n"
    "v_mfma_f32_16x16x32_bf16 v[32:35], v[16:19], v[88:91], v[32:35]\n"
    "v_exp_f32 v160, v160\n"
    "v_cvt_pk_bf16_f32 v72, v0, v1\n"
    "v_cvt_pk_bf16_f32 v73, v2, v3\n"
    "v_cvt_pk_bf16_f32 v74, v160, 0\n"
    "v_mfma_f32_16x16x32_bf16 v[116:119], v[94:97], v[144:147], 0\n"
    "s_cbranch_vccnz .Lx100_%=\n"
    ".Lr100_%=:\n"
    "v_exp_f32 v164, v164\n"
    "v_mfma_f32_16x16x32_bf16 v[116:119], v[60:63], v[72:75], v[116:119]\n"
    "v_exp_f32 v165, v165\n"
    "v_mfma_f32_16x16x32_bf16 v[68:71], v[124:127], v[144:147], 0\n"
    "v_exp_f32 v166, v166\n"
    "v_mfma_f32_16x16x32_bf16 v[68:71], v[98:101], v[72:75], v[68:71]\n"
    "v_exp_f32 v167, v167\n"
    "v_mfma_f32_16x16x32_bf16 v[120:123], v[52:55], v[144:147], 0\n"
    "ds_read_b128 v[20:23], v37 offset:46080\n"
    "v_exp_f32 v152, v152\n"
    "v_mfma_f32_16x16x32_bf16 v[120:123], v[76:79], v[72:75], v[120:123]\n"
    "ds_read_b128 v[148:151], v37 offset:47104\n"
    "v_exp_f32 v153, v153\n"
    "v_mfma_f32_16x16x32_bf16 v[160:163], v[136:139], v[144:147], 0\n"
    "ds_read_b128 v[156:159], v37 offset:48128\n"
    "v_exp_f32 v154, v154\n"
    "v_mfma_f32_16x16x32_bf16 v[160:163], v[56:59], v[72:75], v[160:163]\n"
    "s_setprio 0\n"
    "v_exp_f32 v155, v155\n"
    "v_cvt_pk_bf16_f32 v132, v164, v165\n"
    "v_cvt_pk_bf16_f32 v133, v166, v167\n"
    "v_cvt_pk_bf16_f32 v134, v152, v153\n"
    "v_cvt_pk_bf16_f32 v135, v154, v155\n"
    "v_exp_f32 v64, v64\n"
    "v_exp_f32 v65, v65\n"
    "v_exp_f32 v66, v66\n"
    "s_setprio 2\n"
    "s_waitcnt lgkmcnt(1)\n"
    "v_mfma_f32_16x16x32_bf16 v[20:23], v[8:11], v[90:93], v[20:23]\n"
    "ds_read_b32 v152, v37 offset:58368\n"
    "v_exp_f32 v67, v67\n"
    "v_mfma_f32_16x16x32_bf16 v[148:151], v[24:27], v[90:93], v[148:151]\n"
    "s_waitcnt lgkmcnt(1)\n"
    "v_exp_f32 v32, v32\n"
    "v_mfma_f32_16x16x32_bf16 v[156:159], v[28:31], v[90:93], v[156:159]\n"
    "s_mov_b64 exec, s[52:53]\n"
    "ds_write_b128 v38, v[68:71] offset:768\n"
    "ds_write_b128 v38, v[116:119] offset:832\n"
    "s_mov_b64 exec, s[54:55]\n"
    "ds_write_b128 v38, v[160:163] offset:896\n"
    "ds_write_b128 v38, v[120:123] offset:960\n"
    "s_mov_b64 exec, -1\n"
    "v_mov_b32 v164, 0x27c40\n"
    "s_waitcnt lgkmcnt(4)\n"
    "v_mfma_f32_16x16x32_bf16 v[152:155], v[140:143], v[90:93], v[152:155]\n"
    "v_mov_b32 v119, 1\n"
    "s_cmp_lg_u32 s35, 0\n"
    "s_cselect_b64 exec, 0, 1\n"
    "ds_add_u32 v164, v119 offset:4\n"
    "s_mov_b64 exec, -1\n"
    "v_cvt_pk_bf16_f32 v72, v64, v65\n"
    "v_cvt_pk_bf16_f32 v73, v66, v67\n"
    "v_cvt_pk_bf16_f32 v74, v32, 0\n"
    "v_mfma_f32_16x16x32_bf16 v[64:67], v[94:97], v[132:135], 0\n"
    "s_cbranch_vccnz .Lx110_%=\n"
    ".Lr110_%=:\n"
    "v_exp_f32 v20, v20\n"
    "v_mfma_f32_16x16x32_bf16 v[64:67], v[60:63], v[72:75], v[64:67]\n"
    "ds_read_b128 v[120:123], v37 offset:41984\n"
    "v_exp_f32 v21, v21\n"
    "v_mfma_f32_16x16x32_bf16 v[160:163], v[124:127], v[132:135], 0\n"
    "ds_read_b128 v[0:3], v37 offset:40960\n"
    "ds_read_b32 v166, v80 offset:8\n"
    "v_exp_f32 v22, v22\n"
    "v_mfma_f32_16x16x32_bf16 v[160:163], v[98:101], v[72:75], v[160:163]\n"
    "s_setprio 0\n"
    "ds_read_b128 v[32:35], v37 offset:39936\n"
    "ds_read_b32 v144, v37 offset:57348\n"
    "v_exp_f32 v23, v23\n"
    "v_exp_f32 v148, v148\n"
    "v_exp_f32 v149, v149\n"
    "v_exp_f32 v150, v150\n"
    "v_exp_f32 v151, v151\n"
    "v_cvt_pk_bf16_f32 v132, v20, v21\n"
    "v_cvt_pk_bf16_f32 v133, v22, v23\n"
    "v_cvt_pk_bf16_f32 v134, v148, v149\n"
    "v_cvt_pk_bf16_f32 v135, v150, v151\n"
    "v_rcp_f32 v66, v66\n"
    "v_exp_f32 v156, v156\n"
    "v_exp_f32 v157, v157\n"
    "v_exp_f32 v158, v158\n"
    "v_exp_f32 v159, v159\n"
    "v_exp_f32 v152, v152\n"
    "v_mul_f32 v64, v64, v66\n"
    "s_setprio 2\n"
    "s_waitcnt lgkmcnt(0)\n"
    "v_mfma_f32_16x16x32_bf16 v[32:35], v[4:7], v[110:113], v[32:35]\n"
    "v_mul_f32 v65, v65, v66\n"
    "v_mul_f32 v160, v160, v66\n"
    "v_mfma_f32_16x16x32_bf16 v[0:3], v[128:131], v[110:113], v[0:3]\n"
    "v_mul_f32 v161, v161, v66\n"
    "v_mul_f32 v162, v162, v66\n"
    "v_mfma_f32_16x16x32_bf16 v[120:123], v[12:15], v[110:113], v[120:123]\n"
    "v_mul_f32 v163, v163, v66\n"
    "v_cvt_pk_bf16_f32 v90, v64, v65\n"
    "v_mfma_f32_16x16x32_bf16 v[144:147], v[16:19], v[110:113], v[144:147]\n"
    "v_cvt_pk_bf16_f32 v88, v160, v161\n"
    "v_cvt_pk_bf16_f32 v89, v162, v163\n"
    "v_cvt_pk_bf16_f32 v72, v156, v157\n"
    "v_cvt_pk_bf16_f32 v73, v158, v159\n"
    "v_cvt_pk_bf16_f32 v74, v152, 0\n"
    "v_readfirstlane_b32 s33, v166\n"
    "s_cmp_ge_i32 s33, 3\n"
    "s_cbranch_scc1 .Ldw_%=\n"
    ".Llw_%=:\n"
    "s_sleep 2\n"
    "ds_read_b32 v166, v80 offset:8\n"
    "s_waitcnt lgkmcnt(0)\n"
    "v_readfirstlane_b32 s33, v166\n"
    "s_cmp_ge_i32 s33, 3\n"
    "s_cbranch_scc0 .Llw_%=\n"
    ".Ldw_%=:\n"
    "v_mfma_f32_16x16x32_bf16 v[160:163], v[52:55], v[132:135], 0\n"
    "s_cbranch_vccnz .Lx101_%=\n"
    ".Lr101_%=:\n"
    "v_exp_f32 v32, v32\n"
    "v_mfma_f32_16x16x32_bf16 v[160:163], v[76:79], v[72:75], v[160:163]\n"
    "ds_read_b128 v[156:159], v37 offset:51200\n"
    "v_exp_f32 v33, v33\n"
    "v_mfma_f32_16x16x32_bf16 v[20:23], v[136:139], v[132:135], 0\n"
    "ds_read_b128 v[66:69], v37 offset:50176\n"
    "v_exp_f32 v34, v34\n"
    "v_mfma_f32_16x16x32_bf16 v[20:23], v[56:59], v[72:75], v[20:23]\n"
    "s_setprio 0\n"
    "ds_read_b128 v[132:135], v37 offset:49152\n"
    "ds_read_b32 v152, v37 offset:58372\n"
    "v_exp_f32 v35, v35\n"
    "v_exp_f32 v0, v0\n"
    "v_exp_f32 v1, v1\n"
    "v_exp_f32 v2, v2\n"
    "v_exp_f32 v3, v3\n"
    "v_cvt_pk_bf16_f32 v164, v32, v33\n"
    "v_cvt_pk_bf16_f32 v165, v34, v35\n"
    "v_cvt_pk_bf16_f32 v166, v0, v1\n"
    "v_cvt_pk_bf16_f32 v167, v2, v3\n"
    "v_rcp_f32 v160, v160\n"
    "v_exp_f32 v120, v120\n"
    "v_exp_f32 v121, v121\n"
    "v_exp_f32 v122, v122\n"
    "v_exp_f32 v123, v123\n"
    "v_exp_f32 v144, v144\n"
    "v_mul_f32 v162, v162, v160\n"
    "s_setprio 2\n"
    "s_waitcnt lgkmcnt(0)\n"
    "v_mfma_f32_16x16x32_bf16 v[132:135], v[8:11], v[112:115], v[132:135]\n"
    "v_mul_f32 v163, v163, v160\n"
    "v_mul_f32 v20, v20, v160\n"
    "v_mfma_f32_16x16x32_bf16 v[66:69], v[24:27], v[112:115], v[66:69]\n"
    "v_mul_f32 v21, v21, v160\n"
    "v_mul_f32 v22, v22, v160\n"
    "v_mfma_f32_16x16x32_bf16 v[156:159], v[28:31], v[112:115], v[156:159]\n"
    "v_mul_f32 v23, v23, v160\n"
    "v_cvt_pk_bf16_f32 v91, v162, v163\n"
    "v_mfma_f32_16x16x32_bf16 v[152:155], v[140:143], v[112:115], v[152:155]\n"
    "ds_read_b128 v[116:119], v37 offset:0\n"
    "v_cvt_pk_bf16_f32 v92, v20, v21\n"
    "v_cvt_pk_bf16_f32 v93, v22, v23\n"
    "v_cvt_pk_bf16_f32 v72, v120, v121\n"
    "v_cvt_pk_bf16_f32 v73, v122, v123\n"
    "v_cvt_pk_bf16_f32 v74, v144, 0\n"
    "v_mfma_f32_16x16x32_bf16 v[120:123], v[94:97], v[164:167], 0\n"
    "s_cbranch_vccnz .Lx111_%=\n"
    ".Lr111_%=:\n"
    "ds_read_b128 v[144:147], v37 offset:1024\n"
    "v_exp_f32 v132, v132\n"
    "v_mfma_f32_16x16x32_bf16 v[120:123], v[60:63], v[72:75], v[120:123]\n"
    "ds_read_b128 v[20:23], v37 offset:2048\n"
    "ds_read_b128 v[0:3], v37 offset:3072\n"
    "v_exp_f32 v133, v133\n"
    "v_mfma_f32_16x16x32_bf16 v[148:151], v[124:127], v[164:167], 0\n"
    "ds_read_b128 v[32:35], v37 offset:4096\n"
    "v_exp_f32 v134, v134\n"
    "v_mfma_f32_16x16x32_bf16 v[148:151], v[98:101], v[72:75], v[148:151]\n"
    "s_waitcnt lgkmcnt(4)\n"
    "ds_read_b128 v[164:167], v37 offset:5120\n"
    "v_exp_f32 v135, v135\n"
    "v_exp_f32 v66, v66\n"
    "v_exp_f32 v67, v67\n"
    "v_exp_f32 v68, v68\n"
    "v_mfma_f32_16x16x32_bf16 v[160:163], v[116:119], v[82:85], 0\n"
    "s_waitcnt lgkmcnt(4)\n"
    "ds_read_b128 v[70:73], v37 offset:6144\n"
    "ds_read_b128 v[114:117], v37 offset:7168\n"
    "v_exp_f32 v69, v69\n"
    "v_mfma_f32_16x16x32_bf16 v[160:163], v[144:147], v[86:89], v[160:163]\n"
    "s_waitcnt lgkmcnt(4)\n"
    "ds_read_b128 v[144:147], v37 offset:8192\n"
    "v_cvt_pk_bf16_f32 v64, v132, v133\n"
    "v_cvt_pk_bf16_f32 v65, v134, v135\n"
    "v_mfma_f32_16x16x32_bf16 v[160:163], v[20:23], v[90:93], v[160:163]\n"
    "ds_read_b128 v[132:135], v37 offset:43008\n"
    "v_cvt_pk_bf16_f32 v66, v66, v67\n"
    "v_cvt_pk_bf16_f32 v67, v68, v69\n"
    "v_mfma_f32_16x16x32_bf16 v[20:23], v[0:3], v[82:85], 0\n"
    "s_waitcnt lgkmcnt(4)\n"
    "ds_read_b128 v[0:3], v37 offset:44032\n"
    "v_rcp_f32 v122, v122\n"
    "v_mfma_f32_16x16x32_bf16 v[20:23], v[32:35], v[86:89], v[20:23]\n"
    "ds_read_b128 v[32:35], v37 offset:45056\n"
    "v_exp_f32 v156, v156\n"
    "v_mfma_f32_16x16x32_bf16 v[20:23], v[164:167], v[90:93], v[20:23]\n"
    "s_waitcnt lgkmcnt(4)\n"
    "ds_read_b32 v68, v37 offset:57352\n"
    "v_exp_f32 v157, v157\n"
    "v_mfma_f32_16x16x32_bf16 v[164:167], v[70:73], v[82:85], 0\n"
    "v_exp_f32 v158, v158\n"
    "v_mfma_f32_16x16x32_bf16 v[164:167], v[114:117], v[86:89], v[164:167]\n"
    "s_waitcnt lgkmcnt(4)\n"
    "v_exp_f32 v159, v159\n"
    "v_mfma_f32_16x16x32_bf16 v[164:167], v[144:147], v[90:93], v[164:167]\n"
    "s_waitcnt lgkmcnt(3)\n"
    "v_exp_f32 v152, v152\n"
    "v_mfma_f32_16x16x32_bf16 v[132:135], v[4:7], v[46:49], v[132:135]\n"
    "s_waitcnt lgkmcnt(2)\n"
    "v_mul_f32 v120, v120, v122\n"
    "v_mul_f32 v121, v121, v122\n"
    "v_mfma_f32_16x16x32_bf16 v[0:3], v[128:131], v[46:49], v[0:3]\n"
    "s_waitcnt lgkmcnt(1)\n"
    "v_mul_f32 v148, v148, v122\n"
    "v_mul_f32 v149, v149, v122\n"
    "v_mfma_f32_16x16x32_bf16 v[32:35], v[12:15], v[46:49], v[32:35]\n"
    "s_waitcnt lgkmcnt(0)\n"
    "v_mul_f32 v150, v150, v122\n"
    "v_mul_f32 v151, v151, v122\n"
    "v_mfma_f32_16x16x32_bf16 v[68:71], v[16:19], v[46:49], v[68:71]\n"
    "v_cvt_pk_bf16_f32 v112, v120, v121\n"
    "v_cvt_pk_bf16_f32 v110, v148, v149\n"
    "v_cvt_pk_bf16_f32 v111, v150, v151\n"
    "v_cvt_pk_bf16_f32 v72, v156, v157\n"
    "v_cvt_pk_bf16_f32 v73, v158, v159\n"
    "v_cvt_pk_bf16_f32 v74, v152, 0\n"
    "v_mfma_f32_16x16x32_bf16 v[116:119], v[52:55], v[64:67], 0\n"
    "s_cbranch_vccnz .Lx102_%=\n"
    ".Lr102_%=:\n"
    "ds_read_b128 v[144:147], v37 offset:9216\n"
    "v_exp_f32 v132, v132\n"
    "v_mfma_f32_16x16x32_bf16 v[116:119], v[76:79], v[72:75], v[116:119]\n"
    "ds_read_b128 v[156:159], v37 offset:10240\n"
    "ds_read_b128 v[120:123], v37 offset:11264\n"
    "v_exp_f32 v133, v133\n"
    "v_mfma_f32_16x16x32_bf16 v[16:19], v[136:139], v[64:67], 0\n"
    "ds_read_b128 v[4:7], v37 offset:12288\n"
    "v_exp_f32 v134, v134\n"
    "v_mfma_f32_16x16x32_bf16 v[16:19], v[56:59], v[72:75], v[16:19]\n"
    "s_setprio 0\n"
    "ds_read_b128 v[148:151], v37 offset:13312\n"
    "v_exp_f32 v135, v135\n"
    "v_exp_f32 v0, v0\n"
    "v_exp_f32 v1, v1\n"
    "v_exp_f32 v2, v2\n"
    "v_exp_f32 v3, v3\n"
    "s_setprio 2\n"
    "s_waitcnt lgkmcnt(3)\n"
    "v_mfma_f32_16x16x32_bf16 v[12:15], v[144:147], v[82:85], 0\n"
    "ds_read_b128 v[64:67], v37 offset:14336\n"
    "ds_read_b128 v[70:73], v37 offset:15360\n"
    "v_cvt_pk_bf16_f32 v152, v132, v133\n"
    "v_cvt_pk_bf16_f32 v153, v134, v135\n"
    "v_mfma_f32_16x16x32_bf16 v[12:15], v[156:159], v[86:89], v[12:15]\n"
    "s_waitcnt lgkmcnt(3)\n"
    "ds_read_b128 v[132:135], v37 offset:16384\n"
    "ds_read_b128 v[156:159], v37 offset:17408\n"
    "v_cvt_pk_bf16_f32 v154, v0, v1\n"
    "v_cvt_pk_bf16_f32 v155, v2, v3\n"
    "v_mfma_f32_16x16x32_bf16 v[12:15], v[120:123], v[90:93], v[12:15]\n"
    "ds_read_b128 v[144:147], v37 offset:52224\n"
    "v_rcp_f32 v116, v116\n"
    "v_mfma_f32_16x16x32_bf16 v[120:123], v[4:7], v[82:85], 0\n"
    "s_waitcnt lgkmcnt(5)\n"
    "ds_read_b128 v[0:3], v37 offset:53248\n"
    "v_exp_f32 v32, v32\n"
    "v_mfma_f32_16x16x32_bf16 v[120:123], v[148:151], v[86:89], v[120:123]\n"
    "s_waitcnt lgkmcnt(5)\n"
    "ds_read_b128 v[128:131], v37 offset:54272\n"
    "v_exp_f32 v33, v33\n"
    "v_mfma_f32_16x16x32_bf16 v[120:123], v[64:67], v[90:93], v[120:123]\n"
    "s_waitcnt lgkmcnt(5)\n"
    "ds_read_b32 v148, v37 offset:58376\n"
    "ds_read_b32 v80, v36 offset:56328\n"
    "v_exp_f32 v34, v34\n"
    "v_mfma_f32_16x16x32_bf16 v[4:7], v[70:73], v[82:85], 0\n"
    "s_waitcnt lgkmcnt(5)\n"
    "ds_read_b32 v81, v36 offset:56328\n"
    "ds_read_b64 v[84:85], v36 offset:55304\n"
    "v_exp_f32 v35, v35\n"
    "v_mfma_f32_16x16x32_bf16 v[4:7], v[132:135], v[86:89], v[4:7]\n"
    "ds_read_b32 v83, v36 offset:56332\n"
    "v_exp_f32 v68, v68\n"
    "v_mfma_f32_16x16x32_bf16 v[4:7], v[156:159], v[90:93], v[4:7]\n"
    "s_waitcnt lgkmcnt(6)\n"
    "v_mul_f32 v118, v118, v116\n"
    "v_mul_f32 v119, v119, v116\n"
    "v_mfma_f32_16x16x32_bf16 v[144:147], v[8:11], v[48:51], v[144:147]\n"
    "v_mul_f32 v16, v16, v116\n"
    "v_mul_f32 v17, v17, v116\n"
    "v_mfma_f32_16x16x32_bf16 v[0:3], v[24:27], v[48:51], v[0:3]\n"
    "s_waitcnt lgkmcnt(4)\n"
    "v_mul_f32 v18, v18, v116\n"
    "v_mul_f32 v19, v19, v116\n"
    "v_mfma_f32_16x16x32_bf16 v[128:131], v[28:31], v[48:51], v[128:131]\n"
    "v_cvt_pk_bf16_f32 v113, v118, v119\n"
    "v_cvt_pk_bf16_f32 v114, v16, v17\n"
    "v_mfma_f32_16x16x32_bf16 v[148:151], v[140:143], v[48:51], v[148:151]\n"
    "v_cvt_pk_bf16_f32 v115, v18, v19\n"
    "s_cbranch_vccnz .Lxo_%=\n"
    ".Lro_%=:\n"
    "v_cvt_pk_bf16_f32 v72, v32, v33\n"
    "v_cvt_pk_bf16_f32 v73, v34, v35\n"
    "v_cvt_pk_bf16_f32 v74, v68, 0\n"
    "s_setprio 3\n"
    "v_pk_fma_f32 v[132:133], v[160:161], v[160:161], s[42:43]\n"
    "v_pk_fma_f32 v[66:67], v[162:163], v[162:163], s[42:43]\n"
    "v_pk_fma_f32 v[132:133], v[20:21], v[20:21], v[132:133]\n"
    "v_pk_fma_f32 v[66:67], v[22:23], v[22:23], v[66:67]\n"
    "v_pk_fma_f32 v[132:133], v[164:165], v[164:165], v[132:133]\n"
    "v_pk_fma_f32 v[66:67], v[166:167], v[166:167], v[66:67]\n"
    "v_pk_fma_f32 v[132:133], v[12:13], v[12:13], v[132:133]\n"
    "v_pk_fma_f32 v[66:67], v[14:15], v[14:15], v[66:67]\n"
    "v_pk_fma_f32 v[132:133], v[120:121], v[120:121], v[132:133]\n"
    "v_pk_fma_f32 v[66:67], v[122:123], v[122:123], v[66:67]\n"
    "v_pk_fma_f32 v[132:133], v[4:5], v[4:5], v[132:133]\n"
    "v_pk_fma_f32 v[66:67], v[6:7], v[6:7], v[66:67]\n"
    "s_setprio 1\n"
    "v_cvt_pk_bf16_f32 v134, v132, v133\n"
    "v_cvt_pk_bf16_f32 v135, v66, v67\n"
    "s_waitcnt lgkmcnt(0)\n"
    "s_nop 0\n"
    "v_mfma_f32_16x16x16_bf16 v[16:19], v[80:81], v[134:135], 0\n"
    "v_mfma_f32_16x16x32_bf16 v[64:67], v[94:97], v[152:155], 0\n"
    "s_cbranch_vccnz .Lx112_%=\n"
    ".Lr112_%=:\n"
    "v_exp_f32 v144, v144\n"
    "v_mfma_f32_16x16x32_bf16 v[64:67], v[60:63], v[72:75], v[64:67]\n"
    "v_exp_f32 v145, v145\n"
    "v_mfma_f32_16x16x32_bf16 v[156:159], v[124:127], v[152:155], 0\n"
    "v_exp_f32 v146, v146\n"
    "v_mfma_f32_16x16x32_bf16 v[156:159], v[98:101], v[72:75], v[156:159]\n"
    "s_setprio 0\n"
    "v_exp_f32 v147, v147\n"
    "v_exp_f32 v0, v0\n"
    "v_exp_f32 v1, v1\n"
    "v_exp_f32 v2, v2\n"
    "v_exp_f32 v3, v3\n"
    "v_cvt_pk_bf16_f32 v88, v144, v145\n"
    "v_cvt_pk_bf16_f32 v89, v146, v147\n"
    "v_cvt_pk_bf16_f32 v90, v0, v1\n"
    "v_cvt_pk_bf16_f32 v91, v2, v3\n"
    "v_rcp_f32 v66, v66\n"
    "v_exp_f32 v128, v128\n"
    "v_exp_f32 v129, v129\n"
    "v_exp_f32 v130, v130\n"
    "v_exp_f32 v131, v131\n"
    "v_exp_f32 v148, v148\n"
    "v_mul_f32 v64, v64, v66\n"
    "v_mul_f32 v65, v65, v66\n"
    "v_mul_f32 v156, v156, v66\n"
    "v_mul_f32 v157, v157, v66\n"
    "v_mul_f32 v158, v158, v66\n"
    "v_mul_f32 v159, v159, v66\n"
    "v_cvt_pk_bf16_f32 v48, v64, v65\n"
    "v_cvt_pk_bf16_f32 v46, v156, v157\n"
    "ds_read_b128 v[68:71], v37 offset:0\n"
    "v_cvt_pk_bf16_f32 v47, v158, v159\n"
    "v_rsq_f32 v16, v16\n"
    "s_nop 0\n"
    "v_mul_f32 v16, 0x411cc471, v16\n"
    "v_cvt_pk_bf16_f32 v72, v128, v129\n"
    "v_cvt_pk_bf16_f32 v73, v130, v131\n"
    "v_cvt_pk_bf16_f32 v74, v148, 0\n"
    "s_setprio 3\n"
    "v_pk_mul_f32 v[160:161], v[160:161], v[16:17] op_sel_hi:[1,0]\n"
    "v_pk_mul_f32 v[162:163], v[162:163], v[16:17] op_sel_hi:[1,0]\n"
    "v_pk_mul_f32 v[20:21], v[20:21], v[16:17] op_sel_hi:[1,0]\n"
    "v_pk_mul_f32 v[22:23], v[22:23], v[16:17] op_sel_hi:[1,0]\n"
    "v_pk_mul_f32 v[164:165], v[164:165], v[16:17] op_sel_hi:[1,0]\n"
    "v_pk_mul_f32 v[166:167], v[166:167], v[16:17] op_sel_hi:[1,0]\n"
    "v_pk_mul_f32 v[12:13], v[12:13], v[16:17] op_sel_hi:[1,0]\n"
    "v_pk_mul_f32 v[14:15], v[14:15], v[16:17] op_sel_hi:[1,0]\n"
    "v_pk_mul_f32 v[120:121], v[120:121], v[16:17] op_sel_hi:[1,0]\n"
    "v_pk_mul_f32 v[122:123], v[122:123], v[16:17] op_sel_hi:[1,0]\n"
    "v_pk_mul_f32 v[4:5], v[4:5], v[16:17] op_sel_hi:[1,0]\n"
    "v_pk_mul_f32 v[6:7], v[6:7], v[16:17] op_sel_hi:[1,0]\n"
    "s_setprio 1\n"
    "v_mfma_f32_16x16x32_bf16 v[156:159], v[52:55], v[88:91], 0\n"
    "ds_read_b128 v[144:147], v37 offset:1024\n"
    "ds_read_b128 v[132:135], v37 offset:2048\n"
    "v_cvt_pk_bf16_f32 v160, v160, v161\n"
    "v_cvt_pk_bf16_f32 v161, v162, v163\n"
    "v_mfma_f32_16x16x32_bf16 v[156:159], v[76:79], v[72:75], v[156:159]\n"
    "ds_read_b128 v[116:119], v37 offset:3072\n"
    "v_cvt_pk_bf16_f32 v162, v20, v21\n"
    "v_cvt_pk_bf16_f32 v163, v22, v23\n"
    "v_mfma_f32_16x16x32_bf16 v[20:23], v[136:139], v[88:91], 0\n"
    "v_cvt_pk_bf16_f32 v64, v164, v165\n"
    "v_cvt_pk_bf16_f32 v65, v166, v167\n"
    "v_mfma_f32_16x16x32_bf16 v[20:23], v[56:59], v[72:75], v[20:23]\n"
    "ds_read_b128 v[24:27], v37 offset:4096\n"
    "v_cvt_pk_bf16_f32 v66, v12, v13\n"
    "v_cvt_pk_bf16_f32 v67, v14, v15\n"
    "v_rcp_f32 v156, v156\n"
    "s_nop 0\n"
    "v_mul_f32 v158, v158, v156\n"
    "v_mul_f32 v159, v159, v156\n"
    "s_nop 0\n"
    "v_mul_f32 v20, v20, v156\n"
    "v_mul_f32 v21, v21, v156\n"
    "v_mul_f32 v22, v22, v156\n"
    "v_mul_f32 v23, v23, v156\n"
    "v_cvt_pk_bf16_f32 v49, v158, v159\n"
    "v_cvt_pk_bf16_f32 v50, v20, v21\n"
    "v_cvt_pk_bf16_f32 v51, v22, v23\n"
    "s_waitcnt lgkmcnt(1)\n"
    "v_mfma_f32_16x16x32_bf16 v[20:23], v[68:71], v[104:107], 0\n"
    "ds_read_b128 v[124:127], v37 offset:5120\n"
    "ds_read_b128 v[28:31], v37 offset:6144\n"
    "v_cvt_pk_bf16_f32 v156, v120, v121\n"
    "v_cvt_pk_bf16_f32 v157, v122, v123\n"
    "v_mfma_f32_16x16x32_bf16 v[76:79], v[68:71], v[40:43], 0\n"
    "v_cvt_pk_bf16_f32 v158, v4, v5\n"
    "v_cvt_pk_bf16_f32 v159, v6, v7\n"
    "v_mfma_f32_16x16x32_bf16 v[20:23], v[144:147], v[108:111], v[20:23]\n"
    "ds_read_b128 v[164:167], v37 offset:7168\n"
    "buffer_store_dwordx4 v[160:163], v84, s[16:19], 0 offen offset:0 nt sc1\n"
    "buffer_store_dwordx4 v[64:67], v84, s[16:19], 0 offen offset:64 nt sc1\n"
    "buffer_store_dwordx4 v[156:159], v84, s[16:19], 0 offen offset:128 nt sc1\n"
    "v_mfma_f32_16x16x32_bf16 v[76:79], v[144:147], v[44:47], v[76:79]\n"
    "ds_read_b128 v[136:139], v37 offset:8192\n"
    "v_mfma_f32_16x16x32_bf16 v[20:23], v[132:135], v[112:115], v[20:23]\n"
    "ds_read_b128 v[156:159], v37 offset:9216\n"
    "v_mfma_f32_16x16x32_bf16 v[76:79], v[132:135], v[48:51], v[76:79]\n"
    "v_mfma_f32_16x16x32_bf16 v[140:143], v[116:119], v[104:107], 0\n"
    "ds_read_b128 v[144:147], v37 offset:10240\n"
    "v_mfma_f32_16x16x32_bf16 v[100:103], v[116:119], v[40:43], 0\n"
    "s_waitcnt lgkmcnt(3)\n"
    "v_mfma_f32_16x16x32_bf16 v[140:143], v[24:27], v[108:111], v[140:143]\n"
    "ds_read_b128 v[88:91], v37 offset:11264\n"
    "v_mfma_f32_16x16x32_bf16 v[100:103], v[24:27], v[44:47], v[100:103]\n"
    "v_mfma_f32_16x16x32_bf16 v[140:143], v[124:127], v[112:115], v[140:143]\n"
    "ds_read_b128 v[116:119], v37 offset:12288\n"
    "v_mfma_f32_16x16x32_bf16 v[100:103], v[124:127], v[48:51], v[100:103]\n"
    "v_mfma_f32_16x16x32_bf16 v[0:3], v[28:31], v[104:107], 0\n"
    "ds_read_b128 v[128:131], v37 offset:13312\n"
    "v_mfma_f32_16x16x32_bf16 v[16:19], v[28:31], v[40:43], 0\n"
    "v_mfma_f32_16x16x32_bf16 v[0:3], v[164:167], v[108:111], v[0:3]\n"
    "ds_read_b128 v[148:151], v37 offset:14336\n"
    "v_mfma_f32_16x16x32_bf16 v[16:19], v[164:167], v[44:47], v[16:19]\n"
    "s_waitcnt lgkmcnt(3)\n"
    "v_mfma_f32_16x16x32_bf16 v[0:3], v[136:139], v[112:115], v[0:3]\n"
    "ds_read_b128 v[64:67], v37 offset:15360\n"
    "v_mfma_f32_16x16x32_bf16 v[16:19], v[136:139], v[48:51], v[16:19]\n"
    "v_mfma_f32_16x16x32_bf16 v[72:75], v[156:159], v[104:107], 0\n"
    "ds_read_b128 v[124:127], v37 offset:16384\n"
    "v_mfma_f32_16x16x32_bf16 v[132:135], v[156:159], v[40:43], 0\n"
    "v_mfma_f32_16x16x32_bf16 v[72:75], v[144:147], v[108:111], v[72:75]\n"
    "ds_read_b128 v[92:95], v37 offset:17408\n"
    "v_mfma_f32_16x16x32_bf16 v[132:135], v[144:147], v[44:47], v[132:135]\n"
    "v_mfma_f32_16x16x32_bf16 v[72:75], v[88:91], v[112:115], v[72:75]\n"
    "v_mfma_f32_16x16x32_bf16 v[132:135], v[88:91], v[48:51], v[132:135]\n"
    "s_waitcnt lgkmcnt(2)\n"
    "v_mfma_f32_16x16x32_bf16 v[68:71], v[116:119], v[104:107], 0\n"
    "v_mfma_f32_16x16x32_bf16 v[60:63], v[116:119], v[40:43], 0\n"
    "v_mfma_f32_16x16x32_bf16 v[68:71], v[128:131], v[108:111], v[68:71]\n"
    "v_mfma_f32_16x16x32_bf16 v[60:63], v[128:131], v[44:47], v[60:63]\n"
    "v_mfma_f32_16x16x32_bf16 v[68:71], v[148:151], v[112:115], v[68:71]\n"
    "v_mfma_f32_16x16x32_bf16 v[60:63], v[148:151], v[48:51], v[60:63]\n"
    "v_mfma_f32_16x16x32_bf16 v[148:151], v[64:67], v[104:107], 0\n"
    "v_mfma_f32_16x16x32_bf16 v[136:139], v[64:67], v[40:43], 0\n"
    "s_waitcnt lgkmcnt(0)\n"
    "v_mfma_f32_16x16x32_bf16 v[148:151], v[124:127], v[108:111], v[148:151]\n"
    "v_mfma_f32_16x16x32_bf16 v[136:139], v[124:127], v[44:47], v[136:139]\n"
    "v_mfma_f32_16x16x32_bf16 v[148:151], v[92:95], v[112:115], v[148:151]\n"
    "v_mfma_f32_16x16x32_bf16 v[136:139], v[92:95], v[48:51], v[136:139]\n"
    "s_setprio 3\n"
    "v_pk_fma_f32 v[14:15], v[20:21], v[20:21], s[42:43]\n"
    "v_pk_fma_f32 v[106:107], v[22:23], v[22:23], s[42:43]\n"
    "v_pk_fma_f32 v[14:15], v[140:141], v[140:141], v[14:15]\n"
    "v_pk_fma_f32 v[106:107], v[142:143], v[142:143], v[106:107]\n"
    "v_pk_fma_f32 v[14:15], v[0:1], v[0:1], v[14:15]\n"
    "v_pk_fma_f32 v[106:107], v[2:3], v[2:3], v[106:107]\n"
    "v_pk_fma_f32 v[14:15], v[72:73], v[72:73], v[14:15]\n"
    "v_pk_fma_f32 v[106:107], v[74:75], v[74:75], v[106:107]\n"
    "v_pk_fma_f32 v[14:15], v[68:69], v[68:69], v[14:15]\n"
    "v_pk_fma_f32 v[106:107], v[70:71], v[70:71], v[106:107]\n"
    "v_pk_fma_f32 v[14:15], v[148:149], v[148:149], v[14:15]\n"
    "v_pk_fma_f32 v[106:107], v[150:151], v[150:151], v[106:107]\n"
    "v_pk_fma_f32 v[36:37], v[76:77], v[76:77], s[42:43]\n"
    "v_pk_fma_f32 v[110:111], v[78:79], v[78:79], s[42:43]\n"
    "v_pk_fma_f32 v[36:37], v[100:101], v[100:101], v[36:37]\n"
    "v_pk_fma_f32 v[110:111], v[102:103], v[102:103], v[110:111]\n"
    "v_pk_fma_f32 v[36:37], v[16:17], v[16:17], v[36:37]\n"
    "v_pk_fma_f32 v[110:111], v[18:19], v[18:19], v[110:111]\n"
    "v_pk_fma_f32 v[36:37], v[132:133], v[132:133], v[36:37]\n"
    "v_pk_fma_f32 v[110:111], v[134:135], v[134:135], v[110:111]\n"
    "v_pk_fma_f32 v[36:37], v[60:61], v[60:61], v[36:37]\n"
    "v_pk_fma_f32 v[110:111], v[62:63], v[62:63], v[110:111]\n"
    "v_pk_fma_f32 v[36:37], v[136:137], v[136:137], v[36:37]\n"
    "v_pk_fma_f32 v[110:111], v[138:139], v[138:139], v[110:111]\n"
    "s_setprio 1\n"
    "v_cvt_pk_bf16_f32 v164, v14, v15\n"
    "v_cvt_pk_bf16_f32 v165, v106, v107\n"
    "s_nop 1\n"
    "v_mfma_f32_16x16x16_bf16 v[156:159], v[80:81], v[164:165], 0\n"
    "v_cvt_pk_bf16_f32 v124, v36, v37\n"
    "v_cvt_pk_bf16_f32 v125, v110, v111\n"
    "s_nop 1\n"
    "v_mfma_f32_16x16x16_bf16 v[92:95], v[80:81], v[124:125], 0\n"
    "s_nop 2\n"
    "v_rsq_f32 v156, v156\n"
    "s_nop 0\n"
    "v_mul_f32 v156, 0x411cc471, v156\n"
    "s_nop 1\n"
    "v_rsq_f32 v92, v92\n"
    "s_nop 0\n"
    "v_mul_f32 v92, 0x411cc471, v92\n"
    "s_setprio 3\n"
    "v_pk_mul_f32 v[20:21], v[20:21], v[156:157] op_sel_hi:[1,0]\n"
    "v_pk_mul_f32 v[22:23], v[22:23], v[156:157] op_sel_hi:[1,0]\n"
    "v_pk_mul_f32 v[140:141], v[140:141], v[156:157] op_sel_hi:[1,0]\n"
    "v_pk_mul_f32 v[142:143], v[142:143], v[156:157] op_sel_hi:[1,0]\n"
    "v_pk_mul_f32 v[0:1], v[0:1], v[156:157] op_sel_hi:[1,0]\n"
    "v_pk_mul_f32 v[2:3], v[2:3], v[156:157] op_sel_hi:[1,0]\n"
    "v_pk_mul_f32 v[72:73], v[72:73], v[156:157] op_sel_hi:[1,0]\n"
    "v_pk_mul_f32 v[74:75], v[74:75], v[156:157] op_sel_hi:[1,0]\n"
    "v_pk_mul_f32 v[68:69], v[68:69], v[156:157] op_sel_hi:[1,0]\n"
    "v_pk_mul_f32 v[70:71], v[70:71], v[156:157] op_sel_hi:[1,0]\n"
    "v_pk_mul_f32 v[148:149], v[148:149], v[156:157] op_sel_hi:[1,0]\n"
    "v_pk_mul_f32 v[150:151], v[150:151], v[156:157] op_sel_hi:[1,0]\n"
    "v_pk_mul_f32 v[76:77], v[76:77], v[92:93] op_sel_hi:[1,0]\n"
    "v_pk_mul_f32 v[78:79], v[78:79], v[92:93] op_sel_hi:[1,0]\n"
    "v_pk_mul_f32 v[100:101], v[100:101], v[92:93] op_sel_hi:[1,0]\n"
    "v_pk_mul_f32 v[102:103], v[102:103], v[92:93] op_sel_hi:[1,0]\n"
    "v_pk_mul_f32 v[16:17], v[16:17], v[92:93] op_sel_hi:[1,0]\n"
    "v_pk_mul_f32 v[18:19], v[18:19], v[92:93] op_sel_hi:[1,0]\n"
    "v_pk_mul_f32 v[132:133], v[132:133], v[92:93] op_sel_hi:[1,0]\n"
    "v_pk_mul_f32 v[134:135], v[134:135], v[92:93] op_sel_hi:[1,0]\n"
    "v_pk_mul_f32 v[60:61], v[60:61], v[92:93] op_sel_hi:[1,0]\n"
    "v_pk_mul_f32 v[62:63], v[62:63], v[92:93] op_sel_hi:[1,0]\n"
    "v_pk_mul_f32 v[136:137], v[136:137], v[92:93] op_sel_hi:[1,0]\n"
    "v_pk_mul_f32 v[138:139], v[138:139], v[92:93] op_sel_hi:[1,0]\n"
    "s_setprio 1\n"
    "v_cvt_pk_bf16_f32 v108, v20, v21\n"
    "v_cvt_pk_bf16_f32 v109, v22, v23\n"
    "v_cvt_pk_bf16_f32 v110, v140, v141\n"
    "v_cvt_pk_bf16_f32 v111, v142, v143\n"
    "v_cvt_pk_bf16_f32 v128, v0, v1\n"
    "v_cvt_pk_bf16_f32 v129, v2, v3\n"
    "v_cvt_pk_bf16_f32 v130, v72, v73\n"
    "v_cvt_pk_bf16_f32 v131, v74, v75\n"
    "v_cvt_pk_bf16_f32 v140, v68, v69\n"
    "v_cvt_pk_bf16_f32 v141, v70, v71\n"
    "v_cvt_pk_bf16_f32 v142, v148, v149\n"
    "v_cvt_pk_bf16_f32 v143, v150, v151\n"
    "v_cvt_pk_bf16_f32 v48, v76, v77\n"
    "v_cvt_pk_bf16_f32 v49, v78, v79\n"
    "v_cvt_pk_bf16_f32 v50, v100, v101\n"
    "v_cvt_pk_bf16_f32 v51, v102, v103\n"
    "buffer_store_dwordx4 v[108:111], v85, s[16:19], 0 offen offset:0 nt sc1\n"
    "buffer_store_dwordx4 v[128:131], v85, s[16:19], 0 offen offset:64 nt sc1\n"
    "buffer_store_dwordx4 v[140:143], v85, s[16:19], 0 offen offset:128 nt sc1\n"
    "v_cvt_pk_bf16_f32 v68, v16, v17\n"
    "v_cvt_pk_bf16_f32 v69, v18, v19\n"
    "v_cvt_pk_bf16_f32 v70, v132, v133\n"
    "v_cvt_pk_bf16_f32 v71, v134, v135\n"
    "v_cvt_pk_bf16_f32 v4, v60, v61\n"
    "v_cvt_pk_bf16_f32 v5, v62, v63\n"
    "v_cvt_pk_bf16_f32 v6, v136, v137\n"
    "v_cvt_pk_bf16_f32 v7, v138, v139\n"
    "buffer_store_dwordx4 v[48:51], v83, s[16:19], 0 offen offset:0 nt sc1\n"
    "buffer_store_dwordx4 v[68:71], v83, s[16:19], 0 offen offset:64 nt sc1\n"
    "buffer_store_dwordx4 v[4:7], v83, s[16:19], 0 offen offset:128 nt sc1\n"
    "s_setprio 0\n"
    "s_nop 0\n"
    "s_branch .Lend_%=\n"
    ".Ltails_%=:\n"
    ".Lx0None3_%=:\n"
    "s_nop 7\n"
    "v_mbcnt_lo_u32_b32 v80, -1, 0\n"
    "v_mbcnt_hi_u32_b32 v80, -1, v80\n"
    "v_mov_b32 v81, s34\n"
    "v_lshrrev_b32 v80, 4, v80\n"
    "v_lshl_add_u32 v80, v80, 3, s34\n"
    "ds_read_b64 v[90:91], v80 offset:4608\n"
    "ds_read_b64 v[88:89], v80 offset:4640\n"
    "ds_read_b64 v[50:51], v80 offset:4672\n"
    "ds_read_u16 v81, v81 offset:4800\n"
    "s_waitcnt lgkmcnt(0)\n"
    "v_lshlrev_b32 v80, 16, v90\n"
    "v_and_b32 v90, 0xffff0000, v90\n"
    "v_fmac_f32 v72, 0x3fb8aa3b, v80\n"
    "v_fmac_f32 v73, 0x3fb8aa3b, v90\n"
    "v_lshlrev_b32 v80, 16, v91\n"
    "v_and_b32 v91, 0xffff0000, v91\n"
    "v_fmac_f32 v74, 0x3fb8aa3b, v80\n"
    "v_fmac_f32 v75, 0x3fb8aa3b, v91\n"
    "v_lshlrev_b32 v80, 16, v88\n"
    "v_and_b32 v88, 0xffff0000, v88\n"
    "v_fmac_f32 v112, 0x3fb8aa3b, v80\n"
    "v_fmac_f32 v113, 0x3fb8aa3b, v88\n"
    "v_lshlrev_b32 v80, 16, v89\n"
    "v_and_b32 v89, 0xffff0000, v89\n"
    "v_fmac_f32 v114, 0x3fb8aa3b, v80\n"
    "v_fmac_f32 v115, 0x3fb8aa3b, v89\n"
    "v_lshlrev_b32 v80, 16, v50\n"
    "v_and_b32 v50, 0xffff0000, v50\n"
    "v_fmac_f32 v136, 0x3fb8aa3b, v80\n"
    "v_fmac_f32 v137, 0x3fb8aa3b, v50\n"
    "v_lshlrev_b32 v80, 16, v51\n"
    "v_and_b32 v51, 0xffff0000, v51\n"
    "v_fmac_f32 v138, 0x3fb8aa3b, v80\n"
    "v_fmac_f32 v139, 0x3fb8aa3b, v51\n"
    "v_lshlrev_b32 v81, 16, v81\n"
    "v_fmac_f32 v60, 0x3fb8aa3b, v81\n"
    "v_max3_f32 v80, v72, v73, v74\n"
    "v_max3_f32 v80, v80, v75, v112\n"
    "v_max3_f32 v80, v80, v113, v114\n"
    "v_max3_f32 v80, v80, v115, v136\n"
    "v_max3_f32 v80, v80, v137, v138\n"
    "v_max3_f32 v80, v80, v139, v60\n"
    "v_mov_b32 v81, v80\n"
    "s_nop 1\n"
    "v_permlane16_swap_b32 v80, v81\n"
    "s_nop 1\n"
    "v_max_f32 v80, v80, v81\n"
    "v_mov_b32 v81, v80\n"
    "s_nop 1\n"
    "v_permlane32_swap_b32 v80, v81\n"
    "s_nop 1\n"
    "v_max_f32 v80, v80, v81\n"
    "v_sub_f32 v72, v72, v80\n"
    "v_sub_f32 v73, v73, v80\n"
    "v_sub_f32 v74, v74, v80\n"
    "v_sub_f32 v75, v75, v80\n"
    "v_sub_f32 v112, v112, v80\n"
    "v_sub_f32 v113, v113, v80\n"
    "v_sub_f32 v114, v114, v80\n"
    "v_sub_f32 v115, v115, v80\n"
    "v_sub_f32 v136, v136, v80\n"
    "v_sub_f32 v137, v137, v80\n"
    "v_sub_f32 v138, v138, v80\n"
    "v_sub_f32 v139, v139, v80\n"
    "v_sub_f32 v60, v60, v80\n"
    "s_nop 1\n"
    "s_cbranch_vccnz .Lr0None3_%=\n"
    ".Lx000_%=:\n"
    "s_nop 7\n"
    "v_mbcnt_lo_u32_b32 v138, -1, 0\n"
    "v_mbcnt_hi_u32_b32 v138, -1, v138\n"
    "v_and_b32 v139, 15, v138\n"
    "v_lshl_add_u32 v139, v139, 1, s34\n"
    "v_lshl_add_u32 v138, v138, 3, s34\n"
    "ds_read_b64 v[80:81], v138 offset:0\n"
    "ds_read_b64 v[50:51], v138 offset:512\n"
    "ds_read_b64 v[136:137], v138 offset:1024\n"
    "ds_read_u16 v139, v139 offset:4704\n"
    "s_waitcnt lgkmcnt(0)\n"
    "v_lshlrev_b32 v138, 16, v80\n"
    "v_and_b32 v80, 0xffff0000, v80\n"
    "v_fmac_f32 v124, 0x3fb8aa3b, v138\n"
    "v_fmac_f32 v125, 0x3fb8aa3b, v80\n"
    "v_lshlrev_b32 v138, 16, v81\n"
    "v_and_b32 v81, 0xffff0000, v81\n"
    "v_fmac_f32 v126, 0x3fb8aa3b, v138\n"
    "v_fmac_f32 v127, 0x3fb8aa3b, v81\n"
    "v_lshlrev_b32 v138, 16, v50\n"
    "v_and_b32 v50, 0xffff0000, v50\n"
    "v_fmac_f32 v52, 0x3fb8aa3b, v138\n"
    "v_fmac_f32 v53, 0x3fb8aa3b, v50\n"
    "v_lshlrev_b32 v138, 16, v51\n"
    "v_and_b32 v51, 0xffff0000, v51\n"
    "v_fmac_f32 v54, 0x3fb8aa3b, v138\n"
    "v_fmac_f32 v55, 0x3fb8aa3b, v51\n"
    "v_lshlrev_b32 v138, 16, v136\n"
    "v_and_b32 v136, 0xffff0000, v136\n"
    "v_fmac_f32 v96, 0x3fb8aa3b, v138\n"
    "v_fmac_f32 v97, 0x3fb8aa3b, v136\n"
    "v_lshlrev_b32 v138, 16, v137\n"
    "v_and_b32 v137, 0xffff0000, v137\n"
    "v_fmac_f32 v98, 0x3fb8aa3b, v138\n"
    "v_fmac_f32 v99, 0x3fb8aa3b, v137\n"
    "v_lshlrev_b32 v139, 16, v139\n"
    "v_fmac_f32 v110, 0x3fb8aa3b, v139\n"
    "v_max3_f32 v138, v124, v125, v126\n"
    "v_max3_f32 v138, v138, v127, v52\n"
    "v_max3_f32 v138, v138, v53, v54\n"
    "v_max3_f32 v138, v138, v55, v96\n"
    "v_max3_f32 v138, v138, v97, v98\n"
    "v_max3_f32 v138, v138, v99, v110\n"
    "v_mov_b32 v139, v138\n"
    "s_nop 1\n"
    "v_permlane16_swap_b32 v138, v139\n"
    "s_nop 1\n"
    "v_max_f32 v138, v138, v139\n"
    "v_mov_b32 v139, v138\n"
    "s_nop 1\n"
    "v_permlane32_swap_b32 v138, v139\n"
    "s_nop 1\n"
    "v_max_f32 v138, v138, v139\n"
    "v_sub_f32 v124, v124, v138\n"
    "v_sub_f32 v125, v125, v138\n"
    "v_sub_f32 v126, v126, v138\n"
    "v_sub_f32 v127, v127, v138\n"
    "v_sub_f32 v52, v52, v138\n"
    "v_sub_f32 v53, v53, v138\n"
    "v_sub_f32 v54, v54, v138\n"
    "v_sub_f32 v55, v55, v138\n"
    "v_sub_f32 v96, v96, v138\n"
    "v_sub_f32 v97, v97, v138\n"
    "v_sub_f32 v98, v98, v138\n"
    "v_sub_f32 v99, v99, v138\n"
    "v_sub_f32 v110, v110, v138\n"
    "s_nop 1\n"
    "s_cbranch_vccnz .Lr000_%=\n"
    ".Lx001_%=:\n"
    "s_nop 7\n"
    "v_mbcnt_lo_u32_b32 v138, -1, 0\n"
    "v_mbcnt_hi_u32_b32 v138, -1, v138\n"
    "v_and_b32 v139, 15, v138\n"
    "v_lshl_add_u32 v139, v139, 1, s34\n"
    "v_lshl_add_u32 v138, v138, 3, s34\n"
    "ds_read_b64 v[62:63], v138 offset:1536\n"
    "ds_read_b64 v[110:111], v138 offset:2048\n"
    "ds_read_b64 v[54:55], v138 offset:2560\n"
    "ds_read_u16 v139, v139 offset:4736\n"
    "s_waitcnt lgkmcnt(0)\n"
    "v_lshlrev_b32 v138, 16, v62\n"
    "v_and_b32 v62, 0xffff0000, v62\n"
    "v_fmac_f32 v80, 0x3fb8aa3b, v138\n"
    "v_fmac_f32 v81, 0x3fb8aa3b, v62\n"
    "v_lshlrev_b32 v138, 16, v63\n"
    "v_and_b32 v63, 0xffff0000, v63\n"
    "v_fmac_f32 v82, 0x3fb8aa3b, v138\n"
    "v_fmac_f32 v83, 0x3fb8aa3b, v63\n"
    "v_lshlrev_b32 v138, 16, v110\n"
    "v_and_b32 v110, 0xffff0000, v110\n"
    "v_fmac_f32 v88, 0x3fb8aa3b, v138\n"
    "v_fmac_f32 v89, 0x3fb8aa3b, v110\n"
    "v_lshlrev_b32 v138, 16, v111\n"
    "v_and_b32 v111, 0xffff0000, v111\n"
    "v_fmac_f32 v90, 0x3fb8aa3b, v138\n"
    "v_fmac_f32 v91, 0x3fb8aa3b, v111\n"
    "v_lshlrev_b32 v138, 16, v54\n"
    "v_and_b32 v54, 0xffff0000, v54\n"
    "v_fmac_f32 v164, 0x3fb8aa3b, v138\n"
    "v_fmac_f32 v165, 0x3fb8aa3b, v54\n"
    "v_lshlrev_b32 v138, 16, v55\n"
    "v_and_b32 v55, 0xffff0000, v55\n"
    "v_fmac_f32 v166, 0x3fb8aa3b, v138\n"
    "v_fmac_f32 v167, 0x3fb8aa3b, v55\n"
    "v_lshlrev_b32 v139, 16, v139\n"
    "v_fmac_f32 v50, 0x3fb8aa3b, v139\n"
    "v_max3_f32 v138, v80, v81, v82\n"
    "v_max3_f32 v138, v138, v83, v88\n"
    "v_max3_f32 v138, v138, v89, v90\n"
    "v_max3_f32 v138, v138, v91, v164\n"
    "v_max3_f32 v138, v138, v165, v166\n"
    "v_max3_f32 v138, v138, v167, v50\n"
    "v_mov_b32 v139, v138\n"
    "s_nop 1\n"
    "v_permlane16_swap_b32 v138, v139\n"
    "s_nop 1\n"
    "v_max_f32 v138, v138, v139\n"
    "v_mov_b32 v139, v138\n"
    "s_nop 1\n"
    "v_permlane32_swap_b32 v138, v139\n"
    "s_nop 1\n"
    "v_max_f32 v138, v138, v139\n"
    "v_sub_f32 v80, v80, v138\n"
    "v_sub_f32 v81, v81, v138\n"
    "v_sub_f32 v82, v82, v138\n"
    "v_sub_f32 v83, v83, v138\n"
    "v_sub_f32 v88, v88, v138\n"
    "v_sub_f32 v89, v89, v138\n"
    "v_sub_f32 v90, v90, v138\n"
    "v_sub_f32 v91, v91, v138\n"
    "v_sub_f32 v164, v164, v138\n"
    "v_sub_f32 v165, v165, v138\n"
    "v_sub_f32 v166, v166, v138\n"
    "v_sub_f32 v167, v167, v138\n"
    "v_sub_f32 v50, v50, v138\n"
    "s_nop 1\n"
    "s_cbranch_vccnz .Lr001_%=\n"
    ".Lx002_%=:\n"
    "s_nop 7\n"
    "v_mbcnt_lo_u32_b32 v48, -1, 0\n"
    "v_mbcnt_hi_u32_b32 v48, -1, v48\n"
    "v_and_b32 v49, 15, v48\n"
    "v_lshl_add_u32 v49, v49, 1, s34\n"
    "v_lshl_add_u32 v48, v48, 3, s34\n"
    "ds_read_b64 v[90:91], v48 offset:3072\n"
    "ds_read_b64 v[138:139], v48 offset:3584\n"
    "ds_read_b64 v[40:41], v48 offset:4096\n"
    "ds_read_u16 v49, v49 offset:4768\n"
    "s_waitcnt lgkmcnt(0)\n"
    "v_lshlrev_b32 v48, 16, v90\n"
    "v_and_b32 v90, 0xffff0000, v90\n"
    "v_fmac_f32 v124, 0x3fb8aa3b, v48\n"
    "v_fmac_f32 v125, 0x3fb8aa3b, v90\n"
    "v_lshlrev_b32 v48, 16, v91\n"
    "v_and_b32 v91, 0xffff0000, v91\n"
    "v_fmac_f32 v126, 0x3fb8aa3b, v48\n"
    "v_fmac_f32 v127, 0x3fb8aa3b, v91\n"
    "v_lshlrev_b32 v48, 16, v138\n"
    "v_and_b32 v138, 0xffff0000, v138\n"
    "v_fmac_f32 v72, 0x3fb8aa3b, v48\n"
    "v_fmac_f32 v73, 0x3fb8aa3b, v138\n"
    "v_lshlrev_b32 v48, 16, v139\n"
    "v_and_b32 v139, 0xffff0000, v139\n"
    "v_fmac_f32 v74, 0x3fb8aa3b, v48\n"
    "v_fmac_f32 v75, 0x3fb8aa3b, v139\n"
    "v_lshlrev_b32 v48, 16, v40\n"
    "v_and_b32 v40, 0xffff0000, v40\n"
    "v_fmac_f32 v60, 0x3fb8aa3b, v48\n"
    "v_fmac_f32 v61, 0x3fb8aa3b, v40\n"
    "v_lshlrev_b32 v48, 16, v41\n"
    "v_and_b32 v41, 0xffff0000, v41\n"
    "v_fmac_f32 v62, 0x3fb8aa3b, v48\n"
    "v_fmac_f32 v63, 0x3fb8aa3b, v41\n"
    "v_lshlrev_b32 v49, 16, v49\n"
    "v_fmac_f32 v110, 0x3fb8aa3b, v49\n"
    "v_max3_f32 v48, v124, v125, v126\n"
    "v_max3_f32 v48, v48, v127, v72\n"
    "v_max3_f32 v48, v48, v73, v74\n"
    "v_max3_f32 v48, v48, v75, v60\n"
    "v_max3_f32 v48, v48, v61, v62\n"
    "v_max3_f32 v48, v48, v63, v110\n"
    "v_mov_b32 v49, v48\n"
    "s_nop 1\n"
    "v_permlane16_swap_b32 v48, v49\n"
    "s_nop 1\n"
    "v_max_f32 v48, v48, v49\n"
    "v_mov_b32 v49, v48\n"
    "s_nop 1\n"
    "v_permlane32_swap_b32 v48, v49\n"
    "s_nop 1\n"
    "v_max_f32 v48, v48, v49\n"
    "v_sub_f32 v124, v124, v48\n"
    "v_sub_f32 v125, v125, v48\n"
    "v_sub_f32 v126, v126, v48\n"
    "v_sub_f32 v127, v127, v48\n"
    "v_sub_f32 v72, v72, v48\n"
    "v_sub_f32 v73, v73, v48\n"
    "v_sub_f32 v74, v74, v48\n"
    "v_sub_f32 v75, v75, v48\n"
    "v_sub_f32 v60, v60, v48\n"
    "v_sub_f32 v61, v61, v48\n"
    "v_sub_f32 v62, v62, v48\n"
    "v_sub_f32 v63, v63, v48\n"
    "v_sub_f32 v110, v110, v48\n"
    "s_nop 1\n"
    "s_cbranch_vccnz .Lr002_%=\n"
    ".Lx010_%=:\n"
    "s_nop 7\n"
    "v_mbcnt_lo_u32_b32 v86, -1, 0\n"
    "v_mbcnt_hi_u32_b32 v86, -1, v86\n"
    "v_and_b32 v87, 15, v86\n"
    "v_lshl_add_u32 v87, v87, 1, s34\n"
    "v_lshl_add_u32 v86, v86, 3, s34\n"
    "ds_read_b64 v[110:111], v86 offset:0\n"
    "ds_read_b64 v[80:81], v86 offset:512\n"
    "ds_read_b64 v[50:51], v86 offset:1024\n"
    "ds_read_u16 v87, v87 offset:4704\n"
    "s_waitcnt lgkmcnt(0)\n"
    "v_lshlrev_b32 v86, 16, v110\n"
    "v_and_b32 v110, 0xffff0000, v110\n"
    "v_fmac_f32 v52, 0x3fb8aa3b, v86\n"
    "v_fmac_f32 v53, 0x3fb8aa3b, v110\n"
    "v_lshlrev_b32 v86, 16, v111\n"
    "v_and_b32 v111, 0xffff0000, v111\n"
    "v_fmac_f32 v54, 0x3fb8aa3b, v86\n"
    "v_fmac_f32 v55, 0x3fb8aa3b, v111\n"
    "v_lshlrev_b32 v86, 16, v80\n"
    "v_and_b32 v80, 0xffff0000, v80\n"
    "v_fmac_f32 v96, 0x3fb8aa3b, v86\n"
    "v_fmac_f32 v97, 0x3fb8aa3b, v80\n"
    "v_lshlrev_b32 v86, 16, v81\n"
    "v_and_b32 v81, 0xffff0000, v81\n"
    "v_fmac_f32 v98, 0x3fb8aa3b, v86\n"
    "v_fmac_f32 v99, 0x3fb8aa3b, v81\n"
    "v_lshlrev_b32 v86, 16, v50\n"
    "v_and_b32 v50, 0xffff0000, v50\n"
    "v_fmac_f32 v160, 0x3fb8aa3b, v86\n"
    "v_fmac_f32 v161, 0x3fb8aa3b, v50\n"
    "v_lshlrev_b32 v86, 16, v51\n"
    "v_and_b32 v51, 0xffff0000, v51\n"
    "v_fmac_f32 v162, 0x3fb8aa3b, v86\n"
    "v_fmac_f32 v163, 0x3fb8aa3b, v51\n"
    "v_lshlrev_b32 v87, 16, v87\n"
    "v_fmac_f32 v100, 0x3fb8aa3b, v87\n"
    "v_max3_f32 v86, v52, v53, v54\n"
    "v_max3_f32 v86, v86, v55, v96\n"
    "v_max3_f32 v86, v86, v97, v98\n"
    "v_max3_f32 v86, v86, v99, v160\n"
    "v_max3_f32 v86, v86, v161, v162\n"
    "v_max3_f32 v86, v86, v163, v100\n"
    "v_mov_b32 v87, v86\n"
    "s_nop 1\n"
    "v_permlane16_swap_b32 v86, v87\n"
    "s_nop 1\n"
    "v_max_f32 v86, v86, v87\n"
    "v_mov_b32 v87, v86\n"
    "s_nop 1\n"
    "v_permlane32_swap_b32 v86, v87\n"
    "s_nop 1\n"
    "v_max_f32 v86, v86, v87\n"
    "v_sub_f32 v52, v52, v86\n"
    "v_sub_f32 v53, v53, v86\n"
    "v_sub_f32 v54, v54, v86\n"
    "v_sub_f32 v55, v55, v86\n"
    "v_sub_f32 v96, v96, v86\n"
    "v_sub_f32 v97, v97, v86\n"
    "v_sub_f32 v98, v98, v86\n"
    "v_sub_f32 v99, v99, v86\n"
    "v_sub_f32 v160, v160, v86\n"
    "v_sub_f32 v161, v161, v86\n"
    "v_sub_f32 v162, v162, v86\n"
    "v_sub_f32 v163, v163, v86\n"
    "v_sub_f32 v100, v100, v86\n"
    "s_nop 1\n"
    "s_cbranch_vccnz .Lr010_%=\n"
    ".Lx011_%=:\n"
    "s_nop 7\n"
    "v_mbcnt_lo_u32_b32 v162, -1, 0\n"
    "v_mbcnt_hi_u32_b32 v162, -1, v162\n"
    "v_and_b32 v163, 15, v162\n"
    "v_lshl_add_u32 v163, v163, 1, s34\n"
    "v_lshl_add_u32 v162, v162, 3, s34\n"
    "ds_read_b64 v[80:81], v162 offset:1536\n"
    "ds_read_b64 v[94:95], v162 offset:2048\n"
    "ds_read_b64 v[102:103], v162 offset:2560\n"
    "ds_read_u16 v163, v163 offset:4736\n"
    "s_waitcnt lgkmcnt(0)\n"
    "v_lshlrev_b32 v162, 16, v80\n"
    "v_and_b32 v80, 0xffff0000, v80\n"
    "v_fmac_f32 v156, 0x3fb8aa3b, v162\n"
    "v_fmac_f32 v157, 0x3fb8aa3b, v80\n"
    "v_lshlrev_b32 v162, 16, v81\n"
    "v_and_b32 v81, 0xffff0000, v81\n"
    "v_fmac_f32 v158, 0x3fb8aa3b, v162\n"
    "v_fmac_f32 v159, 0x3fb8aa3b, v81\n"
    "v_lshlrev_b32 v162, 16, v94\n"
    "v_and_b32 v94, 0xffff0000, v94\n"
    "v_fmac_f32 v116, 0x3fb8aa3b, v162\n"
    "v_fmac_f32 v117, 0x3fb8aa3b, v94\n"
    "v_lshlrev_b32 v162, 16, v95\n"
    "v_and_b32 v95, 0xffff0000, v95\n"
    "v_fmac_f32 v118, 0x3fb8aa3b, v162\n"
    "v_fmac_f32 v119, 0x3fb8aa3b, v95\n"
    "v_lshlrev_b32 v162, 16, v102\n"
    "v_and_b32 v102, 0xffff0000, v102\n"
    "v_fmac_f32 v128, 0x3fb8aa3b, v162\n"
    "v_fmac_f32 v129, 0x3fb8aa3b, v102\n"
    "v_lshlrev_b32 v162, 16, v103\n"
    "v_and_b32 v103, 0xffff0000, v103\n"
    "v_fmac_f32 v130, 0x3fb8aa3b, v162\n"
    "v_fmac_f32 v131, 0x3fb8aa3b, v103\n"
    "v_lshlrev_b32 v163, 16, v163\n"
    "v_fmac_f32 v46, 0x3fb8aa3b, v163\n"
    "v_max3_f32 v162, v156, v157, v158\n"
    "v_max3_f32 v162, v162, v159, v116\n"
    "v_max3_f32 v162, v162, v117, v118\n"
    "v_max3_f32 v162, v162, v119, v128\n"
    "v_max3_f32 v162, v162, v129, v130\n"
    "v_max3_f32 v162, v162, v131, v46\n"
    "v_mov_b32 v163, v162\n"
    "s_nop 1\n"
    "v_permlane16_swap_b32 v162, v163\n"
    "s_nop 1\n"
    "v_max_f32 v162, v162, v163\n"
    "v_mov_b32 v163, v162\n"
    "s_nop 1\n"
    "v_permlane32_swap_b32 v162, v163\n"
    "s_nop 1\n"
    "v_max_f32 v162, v162, v163\n"
    "v_sub_f32 v156, v156, v162\n"
    "v_sub_f32 v157, v157, v162\n"
    "v_sub_f32 v158, v158, v162\n"
    "v_sub_f32 v159, v159, v162\n"
    "v_sub_f32 v116, v116, v162\n"
    "v_sub_f32 v117, v117, v162\n"
    "v_sub_f32 v118, v118, v162\n"
    "v_sub_f32 v119, v119, v162\n"
    "v_sub_f32 v128, v128, v162\n"
    "v_sub_f32 v129, v129, v162\n"
    "v_sub_f32 v130, v130, v162\n"
    "v_sub_f32 v131, v131, v162\n"
    "v_sub_f32 v46, v46, v162\n"
    "s_nop 1\n"
    "s_cbranch_vccnz .Lr011_%=\n"
    ".Lx012_%=:\n"
    "s_nop 7\n"
    "v_mbcnt_lo_u32_b32 v90, -1, 0\n"
    "v_mbcnt_hi_u32_b32 v90, -1, v90\n"
    "v_and_b32 v91, 15, v90\n"
    "v_lshl_add_u32 v91, v91, 1, s34\n"
    "v_lshl_add_u32 v90, v90, 3, s34\n"
    "ds_read_b64 v[110:111], v90 offset:3072\n"
    "ds_read_b64 v[98:99], v90 offset:3584\n"
    "ds_read_b64 v[108:109], v90 offset:4096\n"
    "ds_read_u16 v91, v91 offset:4768\n"
    "s_waitcnt lgkmcnt(0)\n"
    "v_lshlrev_b32 v90, 16, v110\n"
    "v_and_b32 v110, 0xffff0000, v110\n"
    "v_fmac_f32 v156, 0x3fb8aa3b, v90\n"
    "v_fmac_f32 v157, 0x3fb8aa3b, v110\n"
    "v_lshlrev_b32 v90, 16, v111\n"
    "v_and_b32 v111, 0xffff0000, v111\n"
    "v_fmac_f32 v158, 0x3fb8aa3b, v90\n"
    "v_fmac_f32 v159, 0x3fb8aa3b, v111\n"
    "v_lshlrev_b32 v90, 16, v98\n"
    "v_and_b32 v98, 0xffff0000, v98\n"
    "v_fmac_f32 v48, 0x3fb8aa3b, v90\n"
    "v_fmac_f32 v49, 0x3fb8aa3b, v98\n"
    "v_lshlrev_b32 v90, 16, v99\n"
    "v_and_b32 v99, 0xffff0000, v99\n"
    "v_fmac_f32 v50, 0x3fb8aa3b, v90\n"
    "v_fmac_f32 v51, 0x3fb8aa3b, v99\n"
    "v_lshlrev_b32 v90, 16, v108\n"
    "v_and_b32 v108, 0xffff0000, v108\n"
    "v_fmac_f32 v116, 0x3fb8aa3b, v90\n"
    "v_fmac_f32 v117, 0x3fb8aa3b, v108\n"
    "v_lshlrev_b32 v90, 16, v109\n"
    "v_and_b32 v109, 0xffff0000, v109\n"
    "v_fmac_f32 v118, 0x3fb8aa3b, v90\n"
    "v_fmac_f32 v119, 0x3fb8aa3b, v109\n"
    "v_lshlrev_b32 v91, 16, v91\n"
    "v_fmac_f32 v94, 0x3fb8aa3b, v91\n"
    "v_max3_f32 v90, v156, v157, v158\n"
    "v_max3_f32 v90, v90, v159, v48\n"
    "v_max3_f32 v90, v90, v49, v50\n"
    "v_max3_f32 v90, v90, v51, v116\n"
    "v_max3_f32 v90, v90, v117, v118\n"
    "v_max3_f32 v90, v90, v119, v94\n"
    "v_mov_b32 v91, v90\n"
    "s_nop 1\n"
    "v_permlane16_swap_b32 v90, v91\n"
    "s_nop 1\n"
    "v_max_f32 v90, v90, v91\n"
    "v_mov_b32 v91, v90\n"
    "s_nop 1\n"
    "v_permlane32_swap_b32 v90, v91\n"
    "s_nop 1\n"
    "v_max_f32 v90, v90, v91\n"
    "v_sub_f32 v156, v156, v90\n"
    "v_sub_f32 v157, v157, v90\n"
    "v_sub_f32 v158, v158, v90\n"
    "v_sub_f32 v159, v159, v90\n"
    "v_sub_f32 v48, v48, v90\n"
    "v_sub_f32 v49, v49, v90\n"
    "v_sub_f32 v50, v50, v90\n"
    "v_sub_f32 v51, v51, v90\n"
    "v_sub_f32 v116, v116, v90\n"
    "v_sub_f32 v117, v117, v90\n"
    "v_sub_f32 v118, v118, v90\n"
    "v_sub_f32 v119, v119, v90\n"
    "v_sub_f32 v94, v94, v90\n"
    "s_nop 1\n"
    "s_cbranch_vccnz .Lr012_%=\n"
    ".Lx1None3_%=:\n"
    "s_nop 7\n"
    "v_mbcnt_lo_u32_b32 v68, -1, 0\n"
    "v_mbcnt_hi_u32_b32 v68, -1, v68\n"
    "v_mov_b32 v69, s34\n"
    "v_lshrrev_b32 v68, 4, v68\n"
    "v_lshl_add_u32 v68, v68, 3, s34\n"
    "ds_read_b64 v[156:157], v68 offset:4608\n"
    "ds_read_b64 v[70:71], v68 offset:4640\n"
    "ds_read_b64 v[148:149], v68 offset:4672\n"
    "ds_read_u16 v69, v69 offset:4800\n"
    "s_waitcnt lgkmcnt(0)\n"
    "v_lshlrev_b32 v68, 16, v156\n"
    "v_and_b32 v156, 0xffff0000, v156\n"
    "v_fmac_f32 v20, 0x3fb8aa3b, v68\n"
    "v_fmac_f32 v21, 0x3fb8aa3b, v156\n"
    "v_lshlrev_b32 v68, 16, v157\n"
    "v_and_b32 v157, 0xffff0000, v157\n"
    "v_fmac_f32 v22, 0x3fb8aa3b, v68\n"
    "v_fmac_f32 v23, 0x3fb8aa3b, v157\n"
    "v_lshlrev_b32 v68, 16, v70\n"
    "v_and_b32 v70, 0xffff0000, v70\n"
    "v_fmac_f32 v120, 0x3fb8aa3b, v68\n"
    "v_fmac_f32 v121, 0x3fb8aa3b, v70\n"
    "v_lshlrev_b32 v68, 16, v71\n"
    "v_and_b32 v71, 0xffff0000, v71\n"
    "v_fmac_f32 v122, 0x3fb8aa3b, v68\n"
    "v_fmac_f32 v123, 0x3fb8aa3b, v71\n"
    "v_lshlrev_b32 v68, 16, v148\n"
    "v_and_b32 v148, 0xffff0000, v148\n"
    "v_fmac_f32 v0, 0x3fb8aa3b, v68\n"
    "v_fmac_f32 v1, 0x3fb8aa3b, v148\n"
    "v_lshlrev_b32 v68, 16, v149\n"
    "v_and_b32 v149, 0xffff0000, v149\n"
    "v_fmac_f32 v2, 0x3fb8aa3b, v68\n"
    "v_fmac_f32 v3, 0x3fb8aa3b, v149\n"
    "v_lshlrev_b32 v69, 16, v69\n"
    "v_fmac_f32 v160, 0x3fb8aa3b, v69\n"
    "v_max3_f32 v68, v20, v21, v22\n"
    "v_max3_f32 v68, v68, v23, v120\n"
    "v_max3_f32 v68, v68, v121, v122\n"
    "v_max3_f32 v68, v68, v123, v0\n"
    "v_max3_f32 v68, v68, v1, v2\n"
    "v_max3_f32 v68, v68, v3, v160\n"
    "v_mov_b32 v69, v68\n"
    "s_nop 1\n"
    "v_permlane16_swap_b32 v68, v69\n"
    "s_nop 1\n"
    "v_max_f32 v68, v68, v69\n"
    "v_mov_b32 v69, v68\n"
    "s_nop 1\n"
    "v_permlane32_swap_b32 v68, v69\n"
    "s_nop 1\n"
    "v_max_f32 v68, v68, v69\n"
    "v_sub_f32 v20, v20, v68\n"
    "v_sub_f32 v21, v21, v68\n"
    "v_sub_f32 v22, v22, v68\n"
    "v_sub_f32 v23, v23, v68\n"
    "v_sub_f32 v120, v120, v68\n"
    "v_sub_f32 v121, v121, v68\n"
    "v_sub_f32 v122, v122, v68\n"
    "v_sub_f32 v123, v123, v68\n"
    "v_sub_f32 v0, v0, v68\n"
    "v_sub_f32 v1, v1, v68\n"
    "v_sub_f32 v2, v2, v68\n"
    "v_sub_f32 v3, v3, v68\n"
    "v_sub_f32 v160, v160, v68\n"
    "s_nop 1\n"
    "s_cbranch_vccnz .Lr1None3_%=\n"
    ".Lx100_%=:\n"
    "s_nop 7\n"
    "v_mbcnt_lo_u32_b32 v148, -1, 0\n"
    "v_mbcnt_hi_u32_b32 v148, -1, v148\n"
    "v_and_b32 v149, 15, v148\n"
    "v_lshl_add_u32 v149, v149, 1, s34\n"
    "v_lshl_add_u32 v148, v148, 3, s34\n"
    "ds_read_b64 v[22:23], v148 offset:0\n"
    "ds_read_b64 v[132:133], v148 offset:512\n"
    "ds_read_b64 v[158:159], v148 offset:1024\n"
    "ds_read_u16 v149, v149 offset:4704\n"
    "s_waitcnt lgkmcnt(0)\n"
    "v_lshlrev_b32 v148, 16, v22\n"
    "v_and_b32 v22, 0xffff0000, v22\n"
    "v_fmac_f32 v164, 0x3fb8aa3b, v148\n"
    "v_fmac_f32 v165, 0x3fb8aa3b, v22\n"
    "v_lshlrev_b32 v148, 16, v23\n"
    "v_and_b32 v23, 0xffff0000, v23\n"
    "v_fmac_f32 v166, 0x3fb8aa3b, v148\n"
    "v_fmac_f32 v167, 0x3fb8aa3b, v23\n"
    "v_lshlrev_b32 v148, 16, v132\n"
    "v_and_b32 v132, 0xffff0000, v132\n"
    "v_fmac_f32 v152, 0x3fb8aa3b, v148\n"
    "v_fmac_f32 v153, 0x3fb8aa3b, v132\n"
    "v_lshlrev_b32 v148, 16, v133\n"
    "v_and_b32 v133, 0xffff0000, v133\n"
    "v_fmac_f32 v154, 0x3fb8aa3b, v148\n"
    "v_fmac_f32 v155, 0x3fb8aa3b, v133\n"
    "v_lshlrev_b32 v148, 16, v158\n"
    "v_and_b32 v158, 0xffff0000, v158\n"
    "v_fmac_f32 v64, 0x3fb8aa3b, v148\n"
    "v_fmac_f32 v65, 0x3fb8aa3b, v158\n"
    "v_lshlrev_b32 v148, 16, v159\n"
    "v_and_b32 v159, 0xffff0000, v159\n"
    "v_fmac_f32 v66, 0x3fb8aa3b, v148\n"
    "v_fmac_f32 v67, 0x3fb8aa3b, v159\n"
    "v_lshlrev_b32 v149, 16, v149\n"
    "v_fmac_f32 v32, 0x3fb8aa3b, v149\n"
    "v_max3_f32 v148, v164, v165, v166\n"
    "v_max3_f32 v148, v148, v167, v152\n"
    "v_max3_f32 v148, v148, v153, v154\n"
    "v_max3_f32 v148, v148, v155, v64\n"
    "v_max3_f32 v148, v148, v65, v66\n"
    "v_max3_f32 v148, v148, v67, v32\n"
    "v_mov_b32 v149, v148\n"
    "s_nop 1\n"
    "v_permlane16_swap_b32 v148, v149\n"
    "s_nop 1\n"
    "v_max_f32 v148, v148, v149\n"
    "v_mov_b32 v149, v148\n"
    "s_nop 1\n"
    "v_permlane32_swap_b32 v148, v149\n"
    "s_nop 1\n"
    "v_max_f32 v148, v148, v149\n"
    "v_sub_f32 v164, v164, v148\n"
    "v_sub_f32 v165, v165, v148\n"
    "v_sub_f32 v166, v166, v148\n"
    "v_sub_f32 v167, v167, v148\n"
    "v_sub_f32 v152, v152, v148\n"
    "v_sub_f32 v153, v153, v148\n"
    "v_sub_f32 v154, v154, v148\n"
    "v_sub_f32 v155, v155, v148\n"
    "v_sub_f32 v64, v64, v148\n"
    "v_sub_f32 v65, v65, v148\n"
    "v_sub_f32 v66, v66, v148\n"
    "v_sub_f32 v67, v67, v148\n"
    "v_sub_f32 v32, v32, v148\n"
    "s_nop 1\n"
    "s_cbranch_vccnz .Lr100_%=\n"
    ".Lx110_%=:\n"
    "s_nop 7\n"
    "v_mbcnt_lo_u32_b32 v68, -1, 0\n"
    "v_mbcnt_hi_u32_b32 v68, -1, v68\n"
    "v_and_b32 v69, 15, v68\n"
    "v_lshl_add_u32 v69, v69, 1, s34\n"
    "v_lshl_add_u32 v68, v68, 3, s34\n"
    "ds_read_b64 v[120:121], v68 offset:0\n"
    "ds_read_b64 v[118:119], v68 offset:512\n"
    "ds_read_b64 v[164:165], v68 offset:1024\n"
    "ds_read_u16 v69, v69 offset:4704\n"
    "s_waitcnt lgkmcnt(0)\n"
    "v_lshlrev_b32 v68, 16, v120\n"
    "v_and_b32 v120, 0xffff0000, v120\n"
    "v_fmac_f32 v20, 0x3fb8aa3b, v68\n"
    "v_fmac_f32 v21, 0x3fb8aa3b, v120\n"
    "v_lshlrev_b32 v68, 16, v121\n"
    "v_and_b32 v121, 0xffff0000, v121\n"
    "v_fmac_f32 v22, 0x3fb8aa3b, v68\n"
    "v_fmac_f32 v23, 0x3fb8aa3b, v121\n"
    "v_lshlrev_b32 v68, 16, v118\n"
    "v_and_b32 v118, 0xffff0000, v118\n"
    "v_fmac_f32 v148, 0x3fb8aa3b, v68\n"
    "v_fmac_f32 v149, 0x3fb8aa3b, v118\n"
    "v_lshlrev_b32 v68, 16, v119\n"
    "v_and_b32 v119, 0xffff0000, v119\n"
    "v_fmac_f32 v150, 0x3fb8aa3b, v68\n"
    "v_fmac_f32 v151, 0x3fb8aa3b, v119\n"
    "v_lshlrev_b32 v68, 16, v164\n"
    "v_and_b32 v164, 0xffff0000, v164\n"
    "v_fmac_f32 v156, 0x3fb8aa3b, v68\n"
    "v_fmac_f32 v157, 0x3fb8aa3b, v164\n"
    "v_lshlrev_b32 v68, 16, v165\n"
    "v_and_b32 v165, 0xffff0000, v165\n"
    "v_fmac_f32 v158, 0x3fb8aa3b, v68\n"
    "v_fmac_f32 v159, 0x3fb8aa3b, v165\n"
    "v_lshlrev_b32 v69, 16, v69\n"
    "v_fmac_f32 v152, 0x3fb8aa3b, v69\n"
    "v_max3_f32 v68, v20, v21, v22\n"
    "v_max3_f32 v68, v68, v23, v148\n"
    "v_max3_f32 v68, v68, v149, v150\n"
    "v_max3_f32 v68, v68, v151, v156\n"
    "v_max3_f32 v68, v68, v157, v158\n"
    "v_max3_f32 v68, v68, v159, v152\n"
    "v_mov_b32 v69, v68\n"
    "s_nop 1\n"
    "v_permlane16_swap_b32 v68, v69\n"
    "s_nop 1\n"
    "v_max_f32 v68, v68, v69\n"
    "v_mov_b32 v69, v68\n"
    "s_nop 1\n"
    "v_permlane32_swap_b32 v68, v69\n"
    "s_nop 1\n"
    "v_max_f32 v68, v68, v69\n"
    "v_sub_f32 v20, v20, v68\n"
    "v_sub_f32 v21, v21, v68\n"
    "v_sub_f32 v22, v22, v68\n"
    "v_sub_f32 v23, v23, v68\n"
    "v_sub_f32 v148, v148, v68\n"
    "v_sub_f32 v149, v149, v68\n"
    "v_sub_f32 v150, v150, v68\n"
    "v_sub_f32 v151, v151, v68\n"
    "v_sub_f32 v156, v156, v68\n"
    "v_sub_f32 v157, v157, v68\n"
    "v_sub_f32 v158, v158, v68\n"
    "v_sub_f32 v159, v159, v68\n"
    "v_sub_f32 v152, v152, v68\n"
    "s_nop 1\n"
    "s_cbranch_vccnz .Lr110_%=\n"
    ".Lx101_%=:\n"
    "s_nop 7\n"
    "v_mbcnt_lo_u32_b32 v64, -1, 0\n"
    "v_mbcnt_hi_u32_b32 v64, -1, v64\n"
    "v_and_b32 v65, 15, v64\n"
    "v_lshl_add_u32 v65, v65, 1, s34\n"
    "v_lshl_add_u32 v64, v64, 3, s34\n"
    "ds_read_b64 v[66:67], v64 offset:1536\n"
    "ds_read_b64 v[156:157], v64 offset:2048\n"
    "ds_read_b64 v[166:167], v64 offset:2560\n"
    "ds_read_u16 v65, v65 offset:4736\n"
    "s_waitcnt lgkmcnt(0)\n"
    "v_lshlrev_b32 v64, 16, v66\n"
    "v_and_b32 v66, 0xffff0000, v66\n"
    "v_fmac_f32 v32, 0x3fb8aa3b, v64\n"
    "v_fmac_f32 v33, 0x3fb8aa3b, v66\n"
    "v_lshlrev_b32 v64, 16, v67\n"
    "v_and_b32 v67, 0xffff0000, v67\n"
    "v_fmac_f32 v34, 0x3fb8aa3b, v64\n"
    "v_fmac_f32 v35, 0x3fb8aa3b, v67\n"
    "v_lshlrev_b32 v64, 16, v156\n"
    "v_and_b32 v156, 0xffff0000, v156\n"
    "v_fmac_f32 v0, 0x3fb8aa3b, v64\n"
    "v_fmac_f32 v1, 0x3fb8aa3b, v156\n"
    "v_lshlrev_b32 v64, 16, v157\n"
    "v_and_b32 v157, 0xffff0000, v157\n"
    "v_fmac_f32 v2, 0x3fb8aa3b, v64\n"
    "v_fmac_f32 v3, 0x3fb8aa3b, v157\n"
    "v_lshlrev_b32 v64, 16, v166\n"
    "v_and_b32 v166, 0xffff0000, v166\n"
    "v_fmac_f32 v120, 0x3fb8aa3b, v64\n"
    "v_fmac_f32 v121, 0x3fb8aa3b, v166\n"
    "v_lshlrev_b32 v64, 16, v167\n"
    "v_and_b32 v167, 0xffff0000, v167\n"
    "v_fmac_f32 v122, 0x3fb8aa3b, v64\n"
    "v_fmac_f32 v123, 0x3fb8aa3b, v167\n"
    "v_lshlrev_b32 v65, 16, v65\n"
    "v_fmac_f32 v144, 0x3fb8aa3b, v65\n"
    "v_max3_f32 v64, v32, v33, v34\n"
    "v_max3_f32 v64, v64, v35, v0\n"
    "v_max3_f32 v64, v64, v1, v2\n"
    "v_max3_f32 v64, v64, v3, v120\n"
    "v_max3_f32 v64, v64, v121, v122\n"
    "v_max3_f32 v64, v64, v123, v144\n"
    "v_mov_b32 v65, v64\n"
    "s_nop 1\n"
    "v_permlane16_swap_b32 v64, v65\n"
    "s_nop 1\n"
    "v_max_f32 v64, v64, v65\n"
    "v_mov_b32 v65, v64\n"
    "s_nop 1\n"
    "v_permlane32_swap_b32 v64, v65\n"
    "s_nop 1\n"
    "v_max_f32 v64, v64, v65\n"
    "v_sub_f32 v32, v32, v64\n"
    "v_sub_f32 v33, v33, v64\n"
    "v_sub_f32 v34, v34, v64\n"
    "v_sub_f32 v35, v35, v64\n"
    "v_sub_f32 v0, v0, v64\n"
    "v_sub_f32 v1, v1, v64\n"
    "v_sub_f32 v2, v2, v64\n"
    "v_sub_f32 v3, v3, v64\n"
    "v_sub_f32 v120, v120, v64\n"
    "v_sub_f32 v121, v121, v64\n"
    "v_sub_f32 v122, v122, v64\n"
    "v_sub_f32 v123, v123, v64\n"
    "v_sub_f32 v144, v144, v64\n"
    "s_nop 1\n"
    "s_cbranch_vccnz .Lr101_%=\n"
    ".Lx111_%=:\n"
    "s_nop 7\n"
    "v_mbcnt_lo_u32_b32 v114, -1, 0\n"
    "v_mbcnt_hi_u32_b32 v114, -1, v114\n"
    "v_and_b32 v115, 15, v114\n"
    "v_lshl_add_u32 v115, v115, 1, s34\n"
    "v_lshl_add_u32 v114, v114, 3, s34\n"
    "ds_read_b64 v[80:81], v114 offset:1536\n"
    "ds_read_b64 v[70:71], v114 offset:2048\n"
    "ds_read_b64 v[64:65], v114 offset:2560\n"
    "ds_read_u16 v115, v115 offset:4736\n"
    "s_waitcnt lgkmcnt(0)\n"
    "v_lshlrev_b32 v114, 16, v80\n"
    "v_and_b32 v80, 0xffff0000, v80\n"
    "v_fmac_f32 v132, 0x3fb8aa3b, v114\n"
    "v_fmac_f32 v133, 0x3fb8aa3b, v80\n"
    "v_lshlrev_b32 v114, 16, v81\n"
    "v_and_b32 v81, 0xffff0000, v81\n"
    "v_fmac_f32 v134, 0x3fb8aa3b, v114\n"
    "v_fmac_f32 v135, 0x3fb8aa3b, v81\n"
    "v_lshlrev_b32 v114, 16, v70\n"
    "v_and_b32 v70, 0xffff0000, v70\n"
    "v_fmac_f32 v66, 0x3fb8aa3b, v114\n"
    "v_fmac_f32 v67, 0x3fb8aa3b, v70\n"
    "v_lshlrev_b32 v114, 16, v71\n"
    "v_and_b32 v71, 0xffff0000, v71\n"
    "v_fmac_f32 v68, 0x3fb8aa3b, v114\n"
    "v_fmac_f32 v69, 0x3fb8aa3b, v71\n"
    "v_lshlrev_b32 v114, 16, v64\n"
    "v_and_b32 v64, 0xffff0000, v64\n"
    "v_fmac_f32 v156, 0x3fb8aa3b, v114\n"
    "v_fmac_f32 v157, 0x3fb8aa3b, v64\n"
    "v_lshlrev_b32 v114, 16, v65\n"
    "v_and_b32 v65, 0xffff0000, v65\n"
    "v_fmac_f32 v158, 0x3fb8aa3b, v114\n"
    "v_fmac_f32 v159, 0x3fb8aa3b, v65\n"
    "v_lshlrev_b32 v115, 16, v115\n"
    "v_fmac_f32 v152, 0x3fb8aa3b, v115\n"
    "v_max3_f32 v114, v132, v133, v134\n"
    "v_max3_f32 v114, v114, v135, v66\n"
    "v_max3_f32 v114, v114, v67, v68\n"
    "v_max3_f32 v114, v114, v69, v156\n"
    "v_max3_f32 v114, v114, v157, v158\n"
    "v_max3_f32 v114, v114, v159, v152\n"
    "v_mov_b32 v115, v114\n"
    "s_nop 1\n"
    "v_permlane16_swap_b32 v114, v115\n"
    "s_nop 1\n"
    "v_max_f32 v114, v114, v115\n"
    "v_mov_b32 v115, v114\n"
    "s_nop 1\n"
    "v_permlane32_swap_b32 v114, v115\n"
    "s_nop 1\n"
    "v_max_f32 v114, v114, v115\n"
    "v_sub_f32 v132, v132, v114\n"
    "v_sub_f32 v133, v133, v114\n"
    "v_sub_f32 v134, v134, v114\n"
    "v_sub_f32 v135, v135, v114\n"
    "v_sub_f32 v66, v66, v114\n"
    "v_sub_f32 v67, v67, v114\n"
    "v_sub_f32 v68, v68, v114\n"
    "v_sub_f32 v69, v69, v114\n"
    "v_sub_f32 v156, v156, v114\n"
    "v_sub_f32 v157, v157, v114\n"
    "v_sub_f32 v158, v158, v114\n"
    "v_sub_f32 v159, v159, v114\n"
    "v_sub_f32 v152, v152, v114\n"
    "s_nop 1\n"
    "s_cbranch_vccnz .Lr111_%=\n"
    ".Lx102_%=:\n"
    "s_nop 7\n"
    "v_mbcnt_lo_u32_b32 v150, -1, 0\n"
    "v_mbcnt_hi_u32_b32 v150, -1, v150\n"
    "v_and_b32 v151, 15, v150\n"
    "v_lshl_add_u32 v151, v151, 1, s34\n"
    "v_lshl_add_u32 v150, v150, 3, s34\n"
    "ds_read_b64 v[148:149], v150 offset:3072\n"
    "ds_read_b64 v[144:145], v150 offset:3584\n"
    "ds_read_b64 v[158:159], v150 offset:4096\n"
    "ds_read_u16 v151, v151 offset:4768\n"
    "s_waitcnt lgkmcnt(0)\n"
    "v_lshlrev_b32 v150, 16, v148\n"
    "v_and_b32 v148, 0xffff0000, v148\n"
    "v_fmac_f32 v132, 0x3fb8aa3b, v150\n"
    "v_fmac_f32 v133, 0x3fb8aa3b, v148\n"
    "v_lshlrev_b32 v150, 16, v149\n"
    "v_and_b32 v149, 0xffff0000, v149\n"
    "v_fmac_f32 v134, 0x3fb8aa3b, v150\n"
    "v_fmac_f32 v135, 0x3fb8aa3b, v149\n"
    "v_lshlrev_b32 v150, 16, v144\n"
    "v_and_b32 v144, 0xffff0000, v144\n"
    "v_fmac_f32 v0, 0x3fb8aa3b, v150\n"
    "v_fmac_f32 v1, 0x3fb8aa3b, v144\n"
    "v_lshlrev_b32 v150, 16, v145\n"
    "v_and_b32 v145, 0xffff0000, v145\n"
    "v_fmac_f32 v2, 0x3fb8aa3b, v150\n"
    "v_fmac_f32 v3, 0x3fb8aa3b, v145\n"
    "v_lshlrev_b32 v150, 16, v158\n"
    "v_and_b32 v158, 0xffff0000, v158\n"
    "v_fmac_f32 v32, 0x3fb8aa3b, v150\n"
    "v_fmac_f32 v33, 0x3fb8aa3b, v158\n"
    "v_lshlrev_b32 v150, 16, v159\n"
    "v_and_b32 v159, 0xffff0000, v159\n"
    "v_fmac_f32 v34, 0x3fb8aa3b, v150\n"
    "v_fmac_f32 v35, 0x3fb8aa3b, v159\n"
    "v_lshlrev_b32 v151, 16, v151\n"
    "v_fmac_f32 v68, 0x3fb8aa3b, v151\n"
    "v_max3_f32 v150, v132, v133, v134\n"
    "v_max3_f32 v150, v150, v135, v0\n"
    "v_max3_f32 v150, v150, v1, v2\n"
    "v_max3_f32 v150, v150, v3, v32\n"
    "v_max3_f32 v150, v150, v33, v34\n"
    "v_max3_f32 v150, v150, v35, v68\n"
    "v_mov_b32 v151, v150\n"
    "s_nop 1\n"
    "v_permlane16_swap_b32 v150, v151\n"
    "s_nop 1\n"
    "v_max_f32 v150, v150, v151\n"
    "v_mov_b32 v151, v150\n"
    "s_nop 1\n"
    "v_permlane32_swap_b32 v150, v151\n"
    "s_nop 1\n"
    "v_max_f32 v150, v150, v151\n"
    "v_sub_f32 v132, v132, v150\n"
    "v_sub_f32 v133, v133, v150\n"
    "v_sub_f32 v134, v134, v150\n"
    "v_sub_f32 v135, v135, v150\n"
    "v_sub_f32 v0, v0, v150\n"
    "v_sub_f32 v1, v1, v150\n"
    "v_sub_f32 v2, v2, v150\n"
    "v_sub_f32 v3, v3, v150\n"
    "v_sub_f32 v32, v32, v150\n"
    "v_sub_f32 v33, v33, v150\n"
    "v_sub_f32 v34, v34, v150\n"
    "v_sub_f32 v35, v35, v150\n"
    "v_sub_f32 v68, v68, v150\n"
    "s_nop 1\n"
    "s_cbranch_vccnz .Lr102_%=\n"
    ".Lxo_%=:\n"
    "s_nop 7\n"
    "s_waitcnt lgkmcnt(0)\n"
    "v_mbcnt_lo_u32_b32 v156, -1, 0\n"
    "v_mbcnt_hi_u32_b32 v156, -1, v156\n"
    "v_and_b32 v156, 15, v156\n"
    "v_add_u32 v157, 0, v156\n"
    "v_mul_u32_u24 v158, 37, v157\n"
    "v_lshrrev_b32 v158, 8, v158\n"
    "v_mul_u32_u24 v159, 7, v158\n"
    "v_sub_u32 v157, v157, v159\n"
    "v_add_u32 v84, s28, v84\n"
    "v_cmp_le_u32 s[48:49], s29, v158\n"
    "v_subrev_u32 v159, 27433728, v84\n"
    "s_nop 1\n"
    "v_cndmask_b32 v84, v84, v159, s[48:49]\n"
    "v_cmp_le_u32 s[48:49], s30, v157\n"
    "v_subrev_u32 v159, 72576, v84\n"
    "s_nop 1\n"
    "v_cndmask_b32 v84, v84, v159, s[48:49]\n"
    "v_add_u32 v157, 16, v156\n"
    "v_mul_u32_u24 v158, 37, v157\n"
    "v_lshrrev_b32 v158, 8, v158\n"
    "v_mul_u32_u24 v159, 7, v158\n"
    "v_sub_u32 v157, v157, v159\n"
    "v_add_u32 v85, s28, v85\n"
    "v_cmp_le_u32 s[48:49], s29, v158\n"
    "v_subrev_u32 v159, 27433728, v85\n"
    "s_nop 1\n"
    "v_cndmask_b32 v85, v85, v159, s[48:49]\n"
    "v_cmp_le_u32 s[48:49], s30, v157\n"
    "v_subrev_u32 v159, 72576, v85\n"
    "s_nop 1\n"
    "v_cndmask_b32 v85, v85, v159, s[48:49]\n"
    "v_add_u32 v157, 32, v156\n"
    "v_mul_u32_u24 v158, 37, v157\n"
    "v_lshrrev_b32 v158, 8, v158\n"
    "v_mul_u32_u24 v159, 7, v158\n"
    "v_sub_u32 v157, v157, v159\n"
    "v_add_u32 v83, s28, v83\n"
    "v_cmp_le_u32 s[48:49], s29, v158\n"
    "v_subrev_u32 v159, 27433728, v83\n"
    "s_nop 1\n"
    "v_cndmask_b32 v83, v83, v159, s[48:49]\n"
    "v_cmp_le_u32 s[48:49], s30, v157\n"
    "v_subrev_u32 v159, 72576, v83\n"
    "s_nop 1\n"
    "v_cndmask_b32 v83, v83, v159, s[48:49]\n"
    "s_nop 1\n"
    "s_cbranch_vccnz .Lro_%=\n"
    ".Lx112_%=:\n"
    "s_nop 7\n"
    "v_mbcnt_lo_u32_b32 v50, -1, 0\n"
    "v_mbcnt_hi_u32_b32 v50, -1, v50\n"
    "v_and_b32 v51, 15, v50\n"
    "v_lshl_add_u32 v51, v51, 1, s34\n"
    "v_lshl_add_u32 v50, v50, 3, s34\n"
    "ds_read_b64 v[86:87], v50 offset:3072\n"
    "ds_read_b64 v[156:157], v50 offset:3584\n"
    "ds_read_b64 v[134:135], v50 offset:4096\n"
    "ds_read_u16 v51, v51 offset:4768\n"
    "s_waitcnt lgkmcnt(0)\n"
    "v_lshlrev_b32 v50, 16, v86\n"
    "v_and_b32 v86, 0xffff0000, v86\n"
    "v_fmac_f32 v144, 0x3fb8aa3b, v50\n"
    "v_fmac_f32 v145, 0x3fb8aa3b, v86\n"
    "v_lshlrev_b32 v50, 16, v87\n"
    "v_and_b32 v87, 0xffff0000, v87\n"
    "v_fmac_f32 v146, 0x3fb8aa3b, v50\n"
    "v_fmac_f32 v147, 0x3fb8aa3b, v87\n"
    "v_lshlrev_b32 v50, 16, v156\n"
    "v_and_b32 v156, 0xffff0000, v156\n"
    "v_fmac_f32 v0, 0x3fb8aa3b, v50\n"
    "v_fmac_f32 v1, 0x3fb8aa3b, v156\n"
    "v_lshlrev_b32 v50, 16, v157\n"
    "v_and_b32 v157, 0xffff0000, v157\n"
    "v_fmac_f32 v2, 0x3fb8aa3b, v50\n"
    "v_fmac_f32 v3, 0x3fb8aa3b, v157\n"
    "v_lshlrev_b32 v50, 16, v134\n"
    "v_and_b32 v134, 0xffff0000, v134\n"
    "v_fmac_f32 v128, 0x3fb8aa3b, v50\n"
    "v_fmac_f32 v129, 0x3fb8aa3b, v134\n"
    "v_lshlrev_b32 v50, 16, v135\n"
    "v_and_b32 v135, 0xffff0000, v135\n"
    "v_fmac_f32 v130, 0x3fb8aa3b, v50\n"
    "v_fmac_f32 v131, 0x3fb8aa3b, v135\n"
    "v_lshlrev_b32 v51, 16, v51\n"
    "v_fmac_f32 v148, 0x3fb8aa3b, v51\n"
    "v_max3_f32 v50, v144, v145, v146\n"
    "v_max3_f32 v50, v50, v147, v0\n"
    "v_max3_f32 v50, v50, v1, v2\n"
    "v_max3_f32 v50, v50, v3, v128\n"
    "v_max3_f32 v50, v50, v129, v130\n"
    "v_max3_f32 v50, v50, v131, v148\n"
    "v_mov_b32 v51, v50\n"
    "s_nop 1\n"
    "v_permlane16_swap_b32 v50, v51\n"
    "s_nop 1\n"
    "v_max_f32 v50, v50, v51\n"
    "v_mov_b32 v51, v50\n"
    "s_nop 1\n"
    "v_permlane32_swap_b32 v50, v51\n"
    "s_nop 1\n"
    "v_max_f32 v50, v50, v51\n"
    "v_sub_f32 v144, v144, v50\n"
    "v_sub_f32 v145, v145, v50\n"
    "v_sub_f32 v146, v146, v50\n"
    "v_sub_f32 v147, v147, v50\n"
    "v_sub_f32 v0, v0, v50\n"
    "v_sub_f32 v1, v1, v50\n"
    "v_sub_f32 v2, v2, v50\n"
    "v_sub_f32 v3, v3, v50\n"
    "v_sub_f32 v128, v128, v50\n"
    "v_sub_f32 v129, v129, v50\n"
    "v_sub_f32 v130, v130, v50\n"
    "v_sub_f32 v131, v131, v50\n"
    "v_sub_f32 v148, v148, v50\n"
    "s_nop 1\n"
    "s_cbranch_vccnz .Lr112_%=\n"
    ".Lend_%=:\n"
    "s_cmp_eq_u32 s31, 2\n"
    "s_cbranch_scc0 .Lnu_%=\n"
    "s_and_b32 s50, %[v], 3\n"
    "s_lshl_b32 s50, s50, 2\n"
    "s_add_i32 s50, s50, 162912\n"
    "v_mov_b32 v40, s50\n"
    "v_mov_b32 v41, 0\n"
    "ds_write_b32 v40, v41\n"
    ".Lnu_%=:\n"
    "s_cmp_lt_u32 s31, 2\n"
    "s_cselect_b32 s50, s35, 0\n"
    "s_cmp_eq_u32 s50, 0\n"
    "s_cbranch_scc0 .Lex_%=\n"
    ".Ldone_%=:\n"
    "s_cmp_lt_u32 %[blk], 27\n"
    "s_cselect_b32 s50, 8, 1\n"
    "s_cmp_eq_u32 %[v], s50\n"
    "s_cbranch_scc0 .Lfin_%=\n"
    "s_waitcnt vmcnt(0) lgkmcnt(0)\n"
    "s_nop 7\n"
    "v_mbcnt_lo_u32_b32 v40, -1, 0\n"
    "v_mbcnt_hi_u32_b32 v40, -1, v40\n"
    "v_and_b32 v138, 63, v40\n"
    "v_and_b32 v60, 15, v40\n"
    "v_bfe_u32 v23, v40, 4, 2\n"
    "v_and_b32 v127, 48, v40\n"
    "s_lshl_b32 s50, %[blk], 2\n"
    "s_mul_i32 s51, %[blk], 12\n"
    "s_sub_i32 s51, s51, 212\n"
    "s_cmp_lt_u32 %[blk], 27\n"
    "s_cselect_b32 s51, s50, s51\n"
    "s_cselect_b32 s60, 0x000f000f, 0\n"
    "s_cselect_b32 s61, 0x000f000f, 0\n"
    "s_cselect_b32 s63, 8, 12\n"
    "s_sub_i32 s51, s51, 4\n"
    "s_nop 0\n"
    "v_add_u32 v79, s51, v60\n"
    "v_add_u32 v14, s50, v60\n"
    "v_mul_u32_u24 v59, 0x1353, v79\n"
    "v_lshrrev_b32 v59, 18, v59\n"
    "v_mul_u32_u24 v163, 53, v59\n"
    "v_sub_u32 v163, v79, v163\n"
    "v_subrev_u32 v21, 54, v14\n"
    "v_min_u32 v21, 53, v21\n"
    "v_min_u32 v159, 53, v14\n"
    "v_cndmask_b32 v21, v59, v21, s[60:61]\n"
    "v_cndmask_b32 v159, v163, v159, s[60:61]\n"
    "v_add_u32 v14, 0xa8e, v14\n"
    "v_cndmask_b32 v79, v79, v14, s[60:61]\n"
    "v_mad_u32_u24 v21, v21, 7, 9\n"
    "v_subrev_u32 v59, 0x17a, v21\n"
    "v_min_u32 v21, v21, v59\n"
    "v_mad_u32_u24 v159, v159, 7, 9\n"
    "v_subrev_u32 v59, 0x17a, v159\n"
    "v_min_u32 v159, v159, v59\n"
    "s_movk_i32 s50, 378\n"
    "s_movk_i32 s51, 192\n"
    "v_mad_u32_u24 v21, v21, s50, v159\n"
    "v_mad_u32_u24 v78, v21, s51, v127\n"
    "s_movk_i32 s50, 2809\n"
    "v_cmp_gt_u32 s[60:61], s63, v60\n"
    "v_cmp_gt_u32 s[48:49], s50, v79\n"
    "s_and_b64 s[60:61], s[60:61], s[48:49]\n"
    "v_mov_b32 v59, 0x7fffff00\n"
    "v_cndmask_b32 v78, v59, v78, s[60:61]\n"
    "s_bcnt1_i32_b64 s63, s[60:61]\n"
    "s_lshr_b32 s63, s63, 2\n"
    "s_movk_i32 s51, 1472\n"
    "v_min_u32 v3, 11, v60\n"
    "v_mad_u32_u24 v3, v3, s51, v127\n"
    "v_add_u32 v3, 0x1ee00, v3\n"
    "v_lshlrev_b32 v165, 4, v138\n"
    "v_add_u32 v165, 0xe000, v165\n"
    "v_mov_b32 v58, 0x3f803f80\n"
    "v_mov_b32 v59, 0x3f803f80\n"
    "s_mov_b32 s16, s94\n"
    "s_and_b32 s17, s95, 0xffff\n"
    "s_mov_b32 s18, 27433728\n"
    "s_mov_b32 s19, 0x20000\n"
    "v_mov_b32 v30, 0x27c40\n"
    ".Lp6_%=:\n"
    "ds_read_b32 v66, v30 offset:8\n"
    "s_waitcnt lgkmcnt(0)\n"
    "v_readfirstlane_b32 s50, v66\n"
    "s_cmp_ge_i32 s50, 3\n"
    "s_cbranch_scc1 .Lq6_%=\n"
    "s_sleep 1\n"
    "s_branch .Lp6_%=\n"
    ".Lq6_%=:\n"
    "ds_read_b128 v[68:71], v165 offset:0\n"
    "ds_read_b128 v[140:143], v165 offset:1024\n"
    "ds_read_b128 v[104:107], v165 offset:2048\n"
    "ds_read_b128 v[116:119], v165 offset:3072\n"
    "ds_read_b128 v[132:135], v165 offset:4096\n"
    "ds_read_b128 v[84:87], v165 offset:5120\n"
    "ds_read_b128 v[96:99], v165 offset:6144\n"
    "ds_read_b128 v[144:147], v165 offset:7168\n"
    "ds_read_b128 v[88:91], v165 offset:8192\n"
    "ds_read_b128 v[80:83], v165 offset:9216\n"
    "ds_read_b128 v[112:115], v165 offset:10240\n"
    "ds_read_b128 v[44:47], v165 offset:11264\n"
    "ds_read_b128 v[40:43], v165 offset:12288\n"
    "ds_read_b128 v[128:131], v165 offset:13312\n"
    "ds_read_b128 v[52:55], v165 offset:14336\n"
    "ds_read_b128 v[64:67], v165 offset:15360\n"
    "ds_read_b128 v[28:31], v165 offset:16384\n"
    "ds_read_b128 v[100:103], v165 offset:17408\n"
    "v_mov_b32 v95, 0x27c40\n"
    ".Lp7_%=:\n"
    "ds_read_b32 v121, v95 offset:4\n"
    "s_waitcnt lgkmcnt(0)\n"
    "v_readfirstlane_b32 s50, v121\n"
    "s_cmp_ge_i32 s50, s63\n"
    "s_cbranch_scc1 .Lq7_%=\n"
    "s_sleep 8\n"
    "s_branch .Lp7_%=\n"
    ".Lq7_%=:\n"
    "s_setprio 0\n"
    "ds_read_b128 v[36:39], v3 offset:64\n"
    "ds_read_b128 v[72:75], v3 offset:192\n"
    "ds_read_b128 v[60:63], v3 offset:320\n"
    "ds_read_b128 v[48:51], v3 offset:448\n"
    "ds_read_b128 v[8:11], v3 offset:0\n"
    "ds_read_b128 v[136:139], v3 offset:128\n"
    "ds_read_b128 v[24:27], v3 offset:256\n"
    "ds_read_b128 v[92:95], v3 offset:384\n"
    "s_waitcnt lgkmcnt(7)\n"
    "v_rcp_f32 v38, v38\n"
    "s_waitcnt lgkmcnt(6)\n"
    "v_rcp_f32 v72, v72\n"
    "s_waitcnt lgkmcnt(5)\n"
    "v_rcp_f32 v62, v62\n"
    "s_waitcnt lgkmcnt(4)\n"
    "v_rcp_f32 v48, v48\n"
    "v_mul_f32 v36, v36, v38\n"
    "v_mul_f32 v37, v37, v38\n"
    "s_waitcnt lgkmcnt(3)\n"
    "v_mul_f32 v8, v8, v38\n"
    "v_mul_f32 v9, v9, v38\n"
    "v_mul_f32 v10, v10, v38\n"
    "v_mul_f32 v11, v11, v38\n"
    "v_cvt_pk_bf16_f32 v154, v36, v37\n"
    "v_cvt_pk_bf16_f32 v152, v8, v9\n"
    "v_cvt_pk_bf16_f32 v153, v10, v11\n"
    "v_mul_f32 v74, v74, v72\n"
    "v_mul_f32 v75, v75, v72\n"
    "s_waitcnt lgkmcnt(2)\n"
    "v_mul_f32 v136, v136, v72\n"
    "v_mul_f32 v137, v137, v72\n"
    "v_mul_f32 v138, v138, v72\n"
    "v_mul_f32 v139, v139, v72\n"
    "v_cvt_pk_bf16_f32 v155, v74, v75\n"
    "v_cvt_pk_bf16_f32 v156, v136, v137\n"
    "v_cvt_pk_bf16_f32 v157, v138, v139\n"
    "v_mul_f32 v60, v60, v62\n"
    "v_mul_f32 v61, v61, v62\n"
    "s_waitcnt lgkmcnt(1)\n"
    "v_mul_f32 v24, v24, v62\n"
    "v_mul_f32 v25, v25, v62\n"
    "v_mul_f32 v26, v26, v62\n"
    "v_mul_f32 v27, v27, v62\n"
    "v_cvt_pk_bf16_f32 v160, v60, v61\n"
    "v_cvt_pk_bf16_f32 v158, v24, v25\n"
    "v_cvt_pk_bf16_f32 v159, v26, v27\n"
    "v_mul_f32 v50, v50, v48\n"
    "v_mul_f32 v51, v51, v48\n"
    "s_waitcnt lgkmcnt(0)\n"
    "v_mul_f32 v92, v92, v48\n"
    "v_mul_f32 v93, v93, v48\n"
    "v_mul_f32 v94, v94, v48\n"
    "v_mul_f32 v95, v95, v48\n"
    "v_cvt_pk_bf16_f32 v161, v50, v51\n"
    "v_cvt_pk_bf16_f32 v162, v92, v93\n"
    "v_cvt_pk_bf16_f32 v163, v94, v95\n"
    "v_mfma_f32_16x16x32_bf16 v[148:151], v[68:71], v[152:155], 0\n"
    "v_mfma_f32_16x16x32_bf16 v[136:139], v[116:119], v[152:155], 0\n"
    "v_mfma_f32_16x16x32_bf16 v[24:27], v[96:99], v[152:155], 0\n"
    "v_mfma_f32_16x16x32_bf16 v[60:63], v[80:83], v[152:155], 0\n"
    "v_mfma_f32_16x16x32_bf16 v[12:15], v[40:43], v[152:155], 0\n"
    "v_mfma_f32_16x16x32_bf16 v[0:3], v[64:67], v[152:155], 0\n"
    "v_mfma_f32_16x16x32_bf16 v[148:151], v[140:143], v[156:159], v[148:151]\n"
    "v_mfma_f32_16x16x32_bf16 v[136:139], v[132:135], v[156:159], v[136:139]\n"
    "v_mfma_f32_16x16x32_bf16 v[24:27], v[144:147], v[156:159], v[24:27]\n"
    "v_mfma_f32_16x16x32_bf16 v[60:63], v[112:115], v[156:159], v[60:63]\n"
    "v_mfma_f32_16x16x32_bf16 v[12:15], v[128:131], v[156:159], v[12:15]\n"
    "v_mfma_f32_16x16x32_bf16 v[0:3], v[28:31], v[156:159], v[0:3]\n"
    "v_mfma_f32_16x16x32_bf16 v[148:151], v[104:107], v[160:163], v[148:151]\n"
    "v_mfma_f32_16x16x32_bf16 v[136:139], v[84:87], v[160:163], v[136:139]\n"
    "v_mfma_f32_16x16x32_bf16 v[24:27], v[88:91], v[160:163], v[24:27]\n"
    "v_mfma_f32_16x16x32_bf16 v[60:63], v[44:47], v[160:163], v[60:63]\n"
    "v_mfma_f32_16x16x32_bf16 v[12:15], v[52:55], v[160:163], v[12:15]\n"
    "v_mfma_f32_16x16x32_bf16 v[0:3], v[100:103], v[160:163], v[0:3]\n"
    "s_nop 2\n"
    "v_fmaak_f32 v20, v148, v148, 0x387ba882\n"
    "v_fmaak_f32 v37, v149, v149, 0x387ba882\n"
    "v_fmaak_f32 v145, v150, v150, 0x387ba882\n"
    "v_fmaak_f32 v49, v151, v151, 0x387ba882\n"
    "v_fmac_f32 v20, v136, v136\n"
    "v_fmac_f32 v37, v137, v137\n"
    "v_fmac_f32 v145, v138, v138\n"
    "v_fmac_f32 v49, v139, v139\n"
    "v_fmac_f32 v20, v24, v24\n"
    "v_fmac_f32 v37, v25, v25\n"
    "v_fmac_f32 v145, v26, v26\n"
    "v_fmac_f32 v49, v27, v27\n"
    "v_fmac_f32 v20, v60, v60\n"
    "v_fmac_f32 v37, v61, v61\n"
    "v_fmac_f32 v145, v62, v62\n"
    "v_fmac_f32 v49, v63, v63\n"
    "v_fmac_f32 v20, v12, v12\n"
    "v_fmac_f32 v37, v13, v13\n"
    "v_fmac_f32 v145, v14, v14\n"
    "v_fmac_f32 v49, v15, v15\n"
    "v_fmac_f32 v20, v0, v0\n"
    "v_fmac_f32 v37, v1, v1\n"
    "v_fmac_f32 v145, v2, v2\n"
    "v_fmac_f32 v49, v3, v3\n"
    "v_cvt_pk_bf16_f32 v66, v20, v37\n"
    "v_cvt_pk_bf16_f32 v67, v145, v49\n"
    "s_nop 1\n"
    "v_mfma_f32_16x16x16_bf16 v[92:95], v[58:59], v[66:67], 0\n"
    "s_nop 7\n"
    "v_rsq_f32 v92, v92\n"
    "s_nop 0\n"
    "v_mul_f32 v92, 0x411cc471, v92\n"
    "v_mul_f32 v148, v148, v92\n"
    "v_mul_f32 v149, v149, v92\n"
    "v_mul_f32 v150, v150, v92\n"
    "v_mul_f32 v151, v151, v92\n"
    "v_mul_f32 v136, v136, v92\n"
    "v_mul_f32 v137, v137, v92\n"
    "v_mul_f32 v138, v138, v92\n"
    "v_mul_f32 v139, v139, v92\n"
    "v_cvt_pk_bf16_f32 v152, v148, v149\n"
    "v_cvt_pk_bf16_f32 v153, v150, v151\n"
    "v_cvt_pk_bf16_f32 v154, v136, v137\n"
    "v_cvt_pk_bf16_f32 v155, v138, v139\n"
    "buffer_store_dwordx4 v[152:155], v78, s[16:19], 0 offen offset:0 nt sc1\n"
    "v_mul_f32 v24, v24, v92\n"
    "v_mul_f32 v25, v25, v92\n"
    "v_mul_f32 v26, v26, v92\n"
    "v_mul_f32 v27, v27, v92\n"
    "v_mul_f32 v60, v60, v92\n"
    "v_mul_f32 v61, v61, v92\n"
    "v_mul_f32 v62, v62, v92\n"
    "v_mul_f32 v63, v63, v92\n"
    "v_cvt_pk_bf16_f32 v24, v24, v25\n"
    "v_cvt_pk_bf16_f32 v25, v26, v27\n"
    "v_cvt_pk_bf16_f32 v26, v60, v61\n"
    "v_cvt_pk_bf16_f32 v27, v62, v63\n"
    "buffer_store_dwordx4 v[24:27], v78, s[16:19], 0 offen offset:64 nt sc1\n"
    "v_mul_f32 v12, v12, v92\n"
    "v_mul_f32 v13, v13, v92\n"
    "v_mul_f32 v14, v14, v92\n"
    "v_mul_f32 v15, v15, v92\n"
    "v_mul_f32 v0, v0, v92\n"
    "v_mul_f32 v1, v1, v92\n"
    "v_mul_f32 v2, v2, v92\n"
    "v_mul_f32 v3, v3, v92\n"
    "v_cvt_pk_bf16_f32 v4, v12, v13\n"
    "v_cvt_pk_bf16_f32 v5, v14, v15\n"
    "v_cvt_pk_bf16_f32 v6, v0, v1\n"
    "v_cvt_pk_bf16_f32 v7, v2, v3\n"
    "buffer_store_dwordx4 v[4:7], v78, s[16:19], 0 offen offset:128 nt sc1\n"
    "s_branch .Lfin_%=\n"
    ".Lslow_%=:\n"
    "s_cmp_eq_u32 s31, 2\n"
    "s_cbranch_scc1 .Lex_%=\n"
    "s_waitcnt vmcnt(0)\n"
    "s_cmp_eq_u32 %[v], 9\n"
    "s_cbranch_scc1 .Lwin_%=\n"
    "s_branch .Ldone_%=\n"
    ".Lex_%=:\n"
    "s_setprio 3\n"
    "s_waitcnt vmcnt(0) lgkmcnt(0)\n"
    "s_nop 7\n"
    "v_mbcnt_lo_u32_b32 v40, -1, 0\n"
    "v_mbcnt_hi_u32_b32 v40, -1, v40\n"
    "v_and_b32 v141, 63, v40\n"
    "v_and_b32 v135, 15, v40\n"
    "v_bfe_u32 v95, v40, 4, 2\n"
    "v_and_b32 v164, 48, v40\n"
    "s_movk_i32 s50, 371\n"
    "s_movk_i32 s51, 192\n"
    "v_add_u32 v1, 0, v135\n"
    "v_mul_u32_u24 v5, 37, v1\n"
    "v_lshrrev_b32 v5, 8, v5\n"
    "v_mad_u32_u24 v5, v5, s50, v1\n"
    "v_mad_u32_u24 v89, v5, s51, v164\n"
    "v_add_u32 v80, 16, v135\n"
    "v_mul_u32_u24 v44, 37, v80\n"
    "v_lshrrev_b32 v44, 8, v44\n"
    "v_mad_u32_u24 v44, v44, s50, v80\n"
    "v_mad_u32_u24 v63, v44, s51, v164\n"
    "v_add_u32 v111, 32, v135\n"
    "v_mul_u32_u24 v139, 37, v111\n"
    "v_lshrrev_b32 v139, 8, v139\n"
    "v_mad_u32_u24 v139, v139, s50, v111\n"
    "v_mad_u32_u24 v114, v139, s51, v164\n"
    "v_add_u32 v53, 0, v135\n"
    "v_mul_u32_u24 v121, 37, v53\n"
    "v_lshrrev_b32 v121, 8, v121\n"
    "v_mul_u32_u24 v100, 7, v121\n"
    "v_sub_u32 v53, v53, v100\n"
    "v_add_u32 v89, s28, v89\n"
    "v_cmp_le_u32 s[48:49], s29, v121\n"
    "v_subrev_u32 v100, 27433728, v89\n"
    "s_nop 1\n"
    "v_cndmask_b32 v89, v89, v100, s[48:49]\n"
    "v_cmp_le_u32 s[48:49], s30, v53\n"
    "v_subrev_u32 v100, 72576, v89\n"
    "s_nop 1\n"
    "v_cndmask_b32 v89, v89, v100, s[48:49]\n"
    "v_add_u32 v1, 16, v135\n"
    "v_mul_u32_u24 v56, 37, v1\n"
    "v_lshrrev_b32 v56, 8, v56\n"
    "v_mul_u32_u24 v99, 7, v56\n"
    "v_sub_u32 v1, v1, v99\n"
    "v_add_u32 v63, s28, v63\n"
    "v_cmp_le_u32 s[48:49], s29, v56\n"
    "v_subrev_u32 v99, 27433728, v63\n"
    "s_nop 1\n"
    "v_cndmask_b32 v63, v63, v99, s[48:49]\n"
    "v_cmp_le_u32 s[48:49], s30, v1\n"
    "v_subrev_u32 v99, 72576, v63\n"
    "s_nop 1\n"
    "v_cndmask_b32 v63, v63, v99, s[48:49]\n"
    "v_add_u32 v72, 32, v135\n"
    "v_mul_u32_u24 v118, 37, v72\n"
    "v_lshrrev_b32 v118, 8, v118\n"
    "v_mul_u32_u24 v41, 7, v118\n"
    "v_sub_u32 v72, v72, v41\n"
    "v_add_u32 v114, s28, v114\n"
    "v_cmp_le_u32 s[48:49], s29, v118\n"
    "v_subrev_u32 v41, 27433728, v114\n"
    "s_nop 1\n"
    "v_cndmask_b32 v114, v114, v41, s[48:49]\n"
    "v_cmp_le_u32 s[48:49], s30, v72\n"
    "v_subrev_u32 v41, 72576, v114\n"
    "s_nop 1\n"
    "v_cndmask_b32 v114, v114, v41, s[48:49]\n"
    "s_mov_b32 s24, s96\n"
    "s_and_b32 s25, s97, 0xffff\n"
    "s_mov_b32 s26, 27433728\n"
    "s_mov_b32 s16, s94\n"
    "s_and_b32 s17, s95, 0xffff\n"
    "s_mov_b32 s18, 27433728\n"
    "s_mov_b32 s52, s38\n"
    "s_and_b32 s53, s39, 0xffff\n"
    "s_mov_b32 s54, 4804\n"
    "s_mov_b32 s55, 0x20000\n"
    "s_mov_b32 s31, 2\n"
    "s_movk_i32 s50, 1568\n"
    "s_movk_i32 s51, 3136\n"
    "s_movk_i32 s60, 4704\n"
    "v_lshlrev_b32 v88, 4, v141\n"
    "v_min_u32 v41, 0x2b0, v88\n"
    "v_mov_b32 v159, 0\n"
    "buffer_load_dwordx4 v[100:103], v88, s[52:55], 0 offen offset:0 nt\n"
    "buffer_load_dwordx4 v[152:155], v88, s[52:55], 0 offen offset:1024 nt\n"
    "buffer_load_dwordx4 v[108:111], v88, s[52:55], 0 offen offset:2048 nt\n"
    "buffer_load_dwordx4 v[96:99], v88, s[52:55], 0 offen offset:3072 nt\n"
    "buffer_load_dwordx4 v[120:123], v41, s[52:55], s37 offen offset:0 nt\n"
    "buffer_load_ushort v149, v159, s[52:55], s37 offen offset:704 nt\n"
    "buffer_load_dwordx4 v[0:3], v89, s[24:27], 0 offen offset:0 nt\n"
    "buffer_load_dwordx4 v[4:7], v89, s[24:27], 0 offen offset:64 nt\n"
    "buffer_load_dwordx4 v[8:11], v89, s[24:27], 0 offen offset:128 nt\n"
    "buffer_load_dwordx4 v[12:15], v63, s[24:27], 0 offen offset:0 nt\n"
    "buffer_load_dwordx4 v[16:19], v63, s[24:27], 0 offen offset:64 nt\n"
    "buffer_load_dwordx4 v[20:23], v63, s[24:27], 0 offen offset:128 nt\n"
    "buffer_load_dwordx4 v[24:27], v114, s[24:27], 0 offen offset:0 nt\n"
    "buffer_load_dwordx4 v[28:31], v114, s[24:27], 0 offen offset:64 nt\n"
    "buffer_load_dwordx4 v[32:35], v114, s[24:27], 0 offen offset:128 nt\n"
    "v_lshrrev_b32 v129, 1, v135\n"
    "v_and_b32 v129, 4, v129\n"
    "v_xor_b32 v129, v129, v135\n"
    "v_xor_b32 v129, v129, v95\n"
    "v_add_u32 v129, v129, v164\n"
    "v_lshlrev_b32 v36, 4, v129\n"
    "v_lshlrev_b32 v37, 4, v141\n"
    "v_add_u32 v37, 0xe000, v37\n"
    "s_mul_i32 s50, %[v], 1472\n"
    "s_add_i32 s50, s50, 125952\n"
    "v_add_u32 v38, s50, v164\n"
    "v_min_u32 v61, 11, v141\n"
    "v_lshl_add_u32 v61, v61, 3, s34\n"
    "v_min_u32 v47, 48, v141\n"
    "v_mul_u32_u24 v105, 98, v47\n"
    "v_add_u32 v105, s34, v105\n"
    "v_lshl_add_u32 v47, v47, 1, s34\n"
    "v_lshl_add_u32 v158, v141, 3, s34\n"
    "v_add_u32 v88, s34, v88\n"
    "v_add_u32 v41, s34, v41\n"
    "v_add_u32 v159, s34, v159\n"
    "v_mul_u32_u24 v59, 98, v135\n"
    "v_lshl_add_u32 v59, v95, 3, v59\n"
    "v_and_b32 v59, -4, v59\n"
    "v_add_u32 v59, s34, v59\n"
    "v_add_u32 v90, 0x620, v59\n"
    "v_add_u32 v89, 0xc40, v59\n"
    "v_and_b32 v136, 1, v135\n"
    "v_lshlrev_b32 v136, 4, v136\n"
    "s_and_b32 s50, %[v], 3\n"
    "s_lshl_b32 s50, s50, 2\n"
    "s_add_i32 s50, s50, 162912\n"
    "v_mov_b32 v163, s50\n"
    "v_mov_b32 v162, 1\n"
    "s_mov_b64 exec, 1\n"
    ".Llk_%=:\n"
    "ds_wrxchg_rtn_b32 v80, v163, v162\n"
    "s_waitcnt lgkmcnt(0)\n"
    "v_readfirstlane_b32 s50, v80\n"
    "s_cmp_eq_u32 s50, 0\n"
    "s_cbranch_scc1 .Llg_%=\n"
    "s_sleep 8\n"
    "s_branch .Llk_%=\n"
    ".Llg_%=:\n"
    "s_mov_b64 exec, -1\n"
    "s_waitcnt vmcnt(14)\n"
    "ds_write_b128 v88, v[100:103] offset:0\n"
    "s_waitcnt vmcnt(13)\n"
    "ds_write_b128 v88, v[152:155] offset:1024\n"
    "s_waitcnt vmcnt(12)\n"
    "ds_write_b128 v88, v[108:111] offset:2048\n"
    "s_waitcnt vmcnt(11)\n"
    "ds_write_b128 v88, v[96:99] offset:3072\n"
    "s_waitcnt vmcnt(10)\n"
    "ds_write_b128 v41, v[120:123] offset:4096\n"
    "s_waitcnt vmcnt(9)\n"
    "ds_write_b16 v159, v149 offset:4800\n"
    "ds_read2_b32 v[160:161], v59 offset0:0 offset1:1\n"
    "ds_read_b32 v127, v59 offset:8\n"
    "ds_read2_b32 v[102:103], v59 offset0:8 offset1:9\n"
    "ds_read_b32 v74, v59 offset:40\n"
    "ds_read2_b32 v[164:165], v59 offset0:16 offset1:17\n"
    "ds_read_b32 v87, v59 offset:72\n"
    "ds_read2_b32 v[64:65], v90 offset0:0 offset1:1\n"
    "ds_read_b32 v92, v90 offset:8\n"
    "ds_read2_b32 v[140:141], v90 offset0:8 offset1:9\n"
    "ds_read_b32 v129, v90 offset:40\n"
    "ds_read2_b32 v[84:85], v90 offset0:16 offset1:17\n"
    "ds_read_b32 v133, v90 offset:72\n"
    "ds_read2_b32 v[68:69], v89 offset0:0 offset1:1\n"
    "ds_read_b32 v81, v89 offset:8\n"
    "ds_read2_b32 v[40:41], v89 offset0:8 offset1:9\n"
    "ds_read_b32 v110, v89 offset:40\n"
    "ds_read2_b32 v[134:135], v89 offset0:16 offset1:17\n"
    "ds_read_b32 v112, v89 offset:72\n"
    "ds_read_b64 v[116:117], v61 offset:4704\n"
    "ds_read_u16 v106, v105 offset:96\n"
    "s_waitcnt lgkmcnt(14)\n"
    "v_alignbit_b32 v160, v161, v160, v136\n"
    "v_alignbit_b32 v161, v127, v161, v136\n"
    "v_alignbit_b32 v102, v103, v102, v136\n"
    "v_alignbit_b32 v103, v74, v103, v136\n"
    "v_alignbit_b32 v164, v165, v164, v136\n"
    "v_alignbit_b32 v165, v87, v165, v136\n"
    "s_waitcnt lgkmcnt(13)\n"
    "v_alignbit_b32 v64, v65, v64, v136\n"
    "s_waitcnt lgkmcnt(12)\n"
    "v_alignbit_b32 v65, v92, v65, v136\n"
    "s_waitcnt lgkmcnt(11)\n"
    "v_alignbit_b32 v140, v141, v140, v136\n"
    "s_waitcnt lgkmcnt(10)\n"
    "v_alignbit_b32 v141, v129, v141, v136\n"
    "s_waitcnt lgkmcnt(9)\n"
    "v_alignbit_b32 v84, v85, v84, v136\n"
    "s_waitcnt lgkmcnt(8)\n"
    "v_alignbit_b32 v85, v133, v85, v136\n"
    "s_waitcnt lgkmcnt(7)\n"
    "v_alignbit_b32 v68, v69, v68, v136\n"
    "s_waitcnt lgkmcnt(6)\n"
    "v_alignbit_b32 v69, v81, v69, v136\n"
    "s_waitcnt lgkmcnt(5)\n"
    "v_alignbit_b32 v40, v41, v40, v136\n"
    "s_waitcnt lgkmcnt(4)\n"
    "v_alignbit_b32 v41, v110, v41, v136\n"
    "s_waitcnt lgkmcnt(3)\n"
    "v_alignbit_b32 v134, v135, v134, v136\n"
    "s_waitcnt lgkmcnt(2)\n"
    "v_alignbit_b32 v135, v112, v135, v136\n"
    "ds_write_b64 v158, v[160:161] offset:0\n"
    "ds_write_b64 v158, v[102:103] offset:512\n"
    "ds_write_b64 v158, v[164:165] offset:1024\n"
    "ds_write_b64 v158, v[64:65] offset:1536\n"
    "ds_write_b64 v158, v[140:141] offset:2048\n"
    "ds_write_b64 v158, v[84:85] offset:2560\n"
    "ds_write_b64 v158, v[68:69] offset:3072\n"
    "ds_write_b64 v158, v[40:41] offset:3584\n"
    "ds_write_b64 v158, v[134:135] offset:4096\n"
    "s_waitcnt lgkmcnt(10)\n"
    "ds_write_b64 v61, v[116:117] offset:4608\n"
    "s_waitcnt lgkmcnt(10)\n"
    "ds_write_b16 v47, v106 offset:4704\n"
    "s_nop 0\n"
    "s_branch .Lwin_%=\n"
    ".Lfin_%=:\n"
        : "+{v40}"(tid)
        : [ka] "s"(__builtin_amdgcn_kernarg_segment_ptr()), [v] "s"(v), [blk] "s"(blk)
        : "memory", "vcc", "scc", "s16", "s17", "s18", "s19", "s20", "s21", "s22", "s23", "s24", "s25", "s26", "s27", "s28", "s29", "s30", "s31", "s33", "s34", "s35", "s36", "s37", "s38", "s39", "s40", "s41", "s42", "s43", "s44", "s45", "s46", "s47", "s48", "s49", "s50", "s51", "s52", "s53", "s54", "s55", "s56", "s57", "s58", "s59", "s60", "s61", "s62", "s63", "s64", "s65", "s66", "s67", "s68", "s69", "s70", "s71", "s72", "s73", "s74", "s75", "s76", "s77", "s78", "s79", "s80", "s81", "s82", "s83", "s84", "s85", "s86", "s87", "s88", "s89", "s90", "s91", "s92", "s93", "s94", "s95", "s96", "s97", "s98", "s99", "v0", "v1", "v2", "v3", "v4", "v5", "v6", "v7", "v8", "v9", "v10", "v11", "v12", "v13", "v14", "v15", "v16", "v17", "v18", "v19", "v20", "v21", "v22", "v23", "v24", "v25", "v26", "v27", "v28", "v29", "v30", "v31", "v32", "v33", "v34", "v35", "v36", "v37", "v38", "v39", "v41", "v42", "v43", "v44", "v45", "v46", "v47", "v48", "v49", "v50", "v51", "v52", "v53", "v54", "v55", "v56", "v57", "v58", "v59", "v60", "v61", "v62", "v63", "v64", "v65", "v66", "v67", "v68", "v69", "v70", "v71", "v72", "v73", "v74", "v75", "v76", "v77", "v78", "v79", "v80", "v81", "v82", "v83", "v84", "v85", "v86", "v87", "v88", "v89", "v90", "v91", "v92", "v93", "v94", "v95", "v96", "v97", "v98", "v99", "v100", "v101", "v102", "v103", "v104", "v105", "v106", "v107", "v108", "v109", "v110", "v111", "v112", "v113", "v114", "v115", "v116", "v117", "v118", "v119", "v120", "v121", "v122", "v123", "v124", "v125", "v126", "v127", "v128", "v129", "v130", "v131", "v132", "v133", "v134", "v135", "v136", "v137", "v138", "v139", "v140", "v141", "v142", "v143", "v144", "v145", "v146", "v147", "v148", "v149", "v150", "v151", "v152", "v153", "v154", "v155", "v156", "v157", "v158", "v159", "v160", "v161", "v162", "v163", "v164", "v165", "v166", "v167");
}

extern "C" void kernel_launch(void* const* d_in, const int* in_sizes, int n_in, void* d_out, int out_size, void* d_ws, size_t ws_size,
                              hipStream_t stream) {
    (void)in_sizes; (void)n_in; (void)out_size; (void)d_ws; (void)ws_size;
    (void)hipFuncSetAttribute((const void*)swin_kernel, hipFuncAttributeMaxDynamicSharedMemorySize, L_TOTAL);
    swin_kernel<<<NBLK, WPB * 64, L_TOTAL, stream>>>(d_in[0], d_in[1], d_in[2], d_in[3], d_in[4], d_in[5], d_in[6], d_in[7], d_in[8], d_out);
}
```
